```python
import torch

def _softmax(x, axis=-1):
    return torch.softmax(x, dim=axis)
def _gelu(x):
    return torch.nn.functional.gelu(x, approximate="tanh")
def _silu(x):
    return torch.nn.functional.silu(x)
def _sigmoid(x):
    return torch.sigmoid(x)
def _layernorm(x, axis=-1, eps=1e-5):
    m = x.mean(dim=axis, keepdim=True)
    v = x.var(dim=axis, keepdim=True, unbiased=False)
    return (x - m) / torch.sqrt(v + eps)
def _scatter_add(tgt, idx, src, axis=0):
    return tgt.clone().index_add_(axis, idx, src)
def _segment_sum(src, seg, n):
    out = torch.zeros((n,) + src.shape[1:], dtype=src.dtype)
    return out.index_add_(0, seg, src)
def _pad(x, pw):
    flat = tuple(v for pair in reversed(pw) for v in pair)
    return torch.nn.functional.pad(x, flat)

kernel_name = 'recurrent_scan__gqa_block_cached__mixer_channel_mlp__decode'

def setup_inputs():

    _w = (lambda a: (lambda t: t.to(torch.bfloat16) if t.is_floating_point() else t)(torch.as_tensor(a)))
    def _A_setup():
        B, Sq, D = 1, 1, 640
        Hq, Hkv, Sk, Dh = 32, 32, 8192, 192
        assert Hq % Hkv == 0, (Hq, Hkv)
        return {
            "x": _w((lambda *s: torch.randn(*s, dtype=torch.bfloat16) * 0.05)(B, Sq, D)),
            "k_cache": _w((lambda *s: torch.randn(*s, dtype=torch.bfloat16) * 1.6)(B, Hkv, Sk, Dh)),
            "v_cache": _w((lambda *s: torch.randn(*s, dtype=torch.bfloat16) * 1.6)(B, Hkv, Sk, Dh)),
            "wq": _w((lambda *s: torch.randn(*s, dtype=torch.bfloat16) * 1.6)(D, Hq * Dh) * (D ** -0.5)),
            "wo": _w((lambda *s: torch.randn(*s, dtype=torch.bfloat16) * 1.6)(Hq * Dh, D) * ((Hq * Dh) ** -0.5)),
        }
    _A_in = _A_setup()

    def _B_setup():
        B, S, D, F = 1, 1, 640, 1536
        return {
            "x": _w((lambda *s: torch.randn(*s, dtype=torch.bfloat16) * 0.05)(B, S, D)),
            "w1": _w((lambda *s: torch.randn(*s, dtype=torch.bfloat16) * 0.8)(D, F) * (D ** -0.5)),
            "b1": _w((lambda *s: torch.randn(*s, dtype=torch.bfloat16) * 0.4)(F)),
            "w2": _w((lambda *s: torch.randn(*s, dtype=torch.bfloat16) * 0.4)(F, D) * (F ** -0.5)),
            "b2": _w((lambda *s: torch.randn(*s, dtype=torch.bfloat16) * 0.2)(D)),
        }
    _B_in = _B_setup()

    return {
        "A_x": _A_in["x"],
        "A_k_cache": _A_in["k_cache"],
        "A_v_cache": _A_in["v_cache"],
        "A_wq": _A_in["wq"],
        "A_wo": _A_in["wo"],
        "B_x": _B_in["x"],
        "B_w1": _B_in["w1"],
        "B_b1": _B_in["b1"],
        "B_w2": _B_in["w2"],
        "B_b2": _B_in["b2"],
    }

def reference(A_x, A_k_cache, A_v_cache, A_wq, A_wo, B_x, B_w1, B_b1, B_w2, B_b2):
    def _A_ref(x, k_cache, v_cache, wq, wo):
        B, Sq, D = x.shape
        Hq, Dh = 32, 192
        Hkv = k_cache.shape[1]
        rep = Hq // Hkv
        q = (lambda x, *d: x.permute(*d))((x @ wq).reshape(B, Sq, Hq, Dh), 0, 2, 1, 3)
        k = (lambda x, n, axis: torch.repeat_interleave(x, n, dim=axis))(k_cache, rep, axis=1)
        v = (lambda x, n, axis: torch.repeat_interleave(x, n, dim=axis))(v_cache, rep, axis=1)
        scores = torch.einsum("bhqd,bhkd->bhqk", q, k) * (Dh ** -0.5)
        p = _softmax(scores, axis=-1)
        o = torch.einsum("bhqk,bhkd->bhqd", p, v)
        return (lambda x, *d: x.permute(*d))(o, 0, 2, 1, 3).reshape(B, Sq, Hq * Dh) @ wo

    def _B_ref(x, w1, b1, w2, b2):
        y = _layernorm(x, axis=-1)
        h = _gelu(torch.einsum("bsd,df->bsf", y, w1) + b1)
        return torch.einsum("bsf,fd->bsd", h, w2) + b2

    x = A_x
    xn = _layernorm(x, axis=-1)
    s = _A_ref(xn, A_k_cache, A_v_cache, A_wq, A_wo)
    g = _silu(_B_ref(xn, B_w1, B_b1, B_w2, B_b2))
    return x + s * g
```

```cpp
#include <hip/hip_runtime.h>
#include <hip/hip_bf16.h>
#include <stdint.h>

constexpr int D = 640, H = 32, SK = 8192, DH = 192, F = 1536, QD = H * DH;
constexpr int NSPLIT = 8, KEYS_PER = SK / NSPLIT;

using u32x4 = __attribute__((ext_vector_type(4))) unsigned;
using bf16x2 = __attribute__((ext_vector_type(2))) __bf16;

__device__ __forceinline__ float bf2f(unsigned short u) { return __builtin_bit_cast(float, (unsigned)u << 16); }
__device__ __forceinline__ float rbf(float f) {
  unsigned u = __builtin_bit_cast(unsigned, f);
  return __builtin_bit_cast(float, u);
}
__device__ __forceinline__ unsigned short f2bf(float f) {
  unsigned u = __builtin_bit_cast(unsigned, f);
  u = (u + 0x7FFFu + ((u >> 16) & 1u)) >> 16;
  return (unsigned short)u;
}
__device__ __forceinline__ float dot2(unsigned k, unsigned q, float acc) {
  return __builtin_amdgcn_fdot2_f32_bf16(__builtin_bit_cast(bf16x2, k), __builtin_bit_cast(bf16x2, q), acc, false);
}
__device__ __forceinline__ float wave_sum(float v) {
  for (int o = 32; o > 0; o >>= 1) v += __shfl_xor(v, o);
  return v;
}

__device__ void block_ln(const float* in, float* out, float* red) {
  int tid = threadIdx.x;
  float s = 0.f;
  for (int i = tid; i < D; i += 256) s += in[i];
  s = wave_sum(s);
  if ((tid & 63) == 0) red[tid >> 6] = s;
  __syncthreads();
  float mean = (red[0] + red[1] + red[2] + red[3]) * (1.0f / D);
  __syncthreads();
  float s2 = 0.f;
  for (int i = tid; i < D; i += 256) { float d = in[i] - mean; s2 += d * d; }
  s2 = wave_sum(s2);
  if ((tid & 63) == 0) red[tid >> 6] = s2;
  __syncthreads();
  float var = (red[0] + red[1] + red[2] + red[3]) * (1.0f / D);
  __syncthreads();
  float mb = rbf(mean), vb = rbf(var);
  float den = rbf(sqrtf(rbf(vb + 1e-5f)));
  for (int i = tid; i < D; i += 256) out[i] = rbf(rbf(in[i] - mb) / den);
  __syncthreads();
}

__global__ void __launch_bounds__(256) k1(const unsigned short* x, const unsigned short* wq, const unsigned short* w1,
                                          const unsigned short* b1, float* qout, float* hout) {
  __shared__ float xs[D], xn[D], yy[D], red[4], part[4][128];
  int tid = threadIdx.x, lane = tid & 63, w = tid >> 6;
  for (int i = tid; i < D; i += 256) xs[i] = bf2f(x[i]);
  __syncthreads();
  block_ln(xs, xn, red);
  int b = blockIdx.x;
  if (b < 48) {
    int col = b * 128 + lane * 2;
    float a0 = 0.f, a1 = 0.f;
    for (int k = w; k < D; k += 4) {
      unsigned u = *(const unsigned*)(wq + (size_t)k * QD + col);
      a0 += xn[k] * bf2f(u & 0xffff); a1 += xn[k] * bf2f(u >> 16);
    }
    part[w][lane * 2] = a0; part[w][lane * 2 + 1] = a1;
    __syncthreads();
    if (tid < 128) qout[b * 128 + tid] = rbf(part[0][tid] + part[1][tid] + part[2][tid] + part[3][tid]);
  } else {
    block_ln(xn, yy, red);
    int cb = (b - 48) * 128;
    int col = cb + lane * 2;
    float a0 = 0.f, a1 = 0.f;
    for (int k = w; k < D; k += 4) {
      unsigned u = *(const unsigned*)(w1 + (size_t)k * F + col);
      a0 += yy[k] * bf2f(u & 0xffff); a1 += yy[k] * bf2f(u >> 16);
    }
    part[w][lane * 2] = a0; part[w][lane * 2 + 1] = a1;
    __syncthreads();
    if (tid < 128) {
      float t = rbf(part[0][tid] + part[1][tid] + part[2][tid] + part[3][tid]);
      t = rbf(t + bf2f(b1[cb + tid]));
      float g = 0.5f * t * (1.0f + tanhf(0.7978845608028654f * (t + 0.044715f * t * t * t)));
      hout[cb + tid] = rbf(g);
    }
  }
}

__global__ void __launch_bounds__(512) k2(const unsigned short* kc, const unsigned short* vc, const float* q,
                                          float* pm, float* pl, float* po) {
  __shared__ float sm[8], sl[8], sacc[8][DH];
  int s = blockIdx.x, h = blockIdx.y;
  int tid = threadIdx.x, lane = tid & 63, w = tid >> 6, r = lane >> 3, c = lane & 7;
  unsigned qp[12];
  for (int seg = 0; seg < 3; ++seg)
    for (int j = 0; j < 4; ++j) {
      int d = seg * 64 + c * 8 + j * 2;
      qp[seg * 4 + j] = (unsigned)f2bf(q[h * DH + d]) | ((unsigned)f2bf(q[h * DH + d + 1]) << 16);
    }
  float m = -INFINITY, l = 0.f, acc[24];
  for (int i = 0; i < 24; ++i) acc[i] = 0.f;
  const float scale = 0.07216878364870322f;
  size_t base = ((size_t)h * SK + (size_t)s * KEYS_PER + w * 128) * DH;
  for (int it = 0; it < 16; ++it) {
    size_t off = base + (size_t)(it * 8 + r) * DH + c * 8;
    u32x4 kk[3], vv[3];
    for (int seg = 0; seg < 3; ++seg) kk[seg] = __builtin_nontemporal_load((const u32x4*)(kc + off + seg * 64));
    for (int seg = 0; seg < 3; ++seg) vv[seg] = __builtin_nontemporal_load((const u32x4*)(vc + off + seg * 64));
    float d = 0.f;
    for (int seg = 0; seg < 3; ++seg) for (int j = 0; j < 4; ++j) d = dot2(kk[seg][j], qp[seg * 4 + j], d);
    d += __shfl_xor(d, 1); d += __shfl_xor(d, 2); d += __shfl_xor(d, 4);
    float sc = d * scale;
    float mn = fmaxf(m, sc);
    float al = __expf(m - mn), p = __expf(sc - mn);
    l = l * al + p; m = mn;
    for (int seg = 0; seg < 3; ++seg) for (int j = 0; j < 4; ++j) {
      int i = seg * 8 + j * 2;
      acc[i] = acc[i] * al + p * bf2f(vv[seg][j] & 0xffff);
      acc[i + 1] = acc[i + 1] * al + p * bf2f(vv[seg][j] >> 16);
    }
  }
  float M = m;
  M = fmaxf(M, __shfl_xor(M, 8)); M = fmaxf(M, __shfl_xor(M, 16)); M = fmaxf(M, __shfl_xor(M, 32));
  float f = __expf(m - M);
  l *= f;
  l += __shfl_xor(l, 8); l += __shfl_xor(l, 16); l += __shfl_xor(l, 32);
  for (int i = 0; i < 24; ++i) { float a = acc[i] * f; a += __shfl_xor(a, 8); a += __shfl_xor(a, 16); a += __shfl_xor(a, 32); acc[i] = a; }
  if (lane < 8) {
    for (int seg = 0; seg < 3; ++seg) for (int j = 0; j < 8; ++j) sacc[w][seg * 64 + c * 8 + j] = acc[seg * 8 + j];
    if (lane == 0) { sm[w] = M; sl[w] = l; }
  }
  __syncthreads();
  if (tid < DH) {
    float MM = -INFINITY;
    for (int i = 0; i < 8; ++i) MM = fmaxf(MM, sm[i]);
    float L = 0.f, o = 0.f;
    for (int i = 0; i < 8; ++i) { float e = __expf(sm[i] - MM); L += e * sl[i]; o += e * sacc[i][tid]; }
    int slot = h * NSPLIT + s;
    po[slot * DH + tid] = o;
    if (tid == 0) { pm[slot] = MM; pl[slot] = L; }
  }
}

__global__ void __launch_bounds__(1024) k3(const unsigned short* x, const unsigned short* wo, const unsigned short* w2,
                                           const unsigned short* b2, const float* pm, const float* pl, const float* po,
                                           const float* hh, unsigned short* out) {
  __shared__ float o[QD], hs[F], part[16][64], part2[16][64];
  int tid = threadIdx.x, lane = tid & 63, w = tid >> 6;
  for (int i = tid; i < QD; i += 1024) {
    int h = i / DH, d = i % DH;
    float MM = -INFINITY;
    for (int s = 0; s < NSPLIT; ++s) MM = fmaxf(MM, pm[h * NSPLIT + s]);
    float L = 0.f, a = 0.f;
    for (int s = 0; s < NSPLIT; ++s) { float e = __expf(pm[h * NSPLIT + s] - MM); L += e * pl[h * NSPLIT + s]; a += e * po[(h * NSPLIT + s) * DH + d]; }
    o[i] = rbf(a / L);
  }
  for (int i = tid; i < F; i += 1024) hs[i] = hh[i];
  __syncthreads();
  int col = blockIdx.x * 64 + lane;
  float a = 0.f;
  for (int k = w; k < QD; k += 16) a += o[k] * bf2f(wo[(size_t)k * D + col]);
  float a2 = 0.f;
  for (int k = w; k < F; k += 16) a2 += hs[k] * bf2f(w2[(size_t)k * D + col]);
  part[w][lane] = a; part2[w][lane] = a2;
  __syncthreads();
  if (tid < 64) {
    float s = 0.f, t = 0.f;
    for (int i = 0; i < 16; ++i) { s += part[i][tid]; t += part2[i][tid]; }
    s = rbf(s);
    t = rbf(rbf(t) + bf2f(b2[col]));
    float g = rbf(t / (1.0f + __expf(-t)));
    float r = rbf(bf2f(x[col]) + rbf(s * g));
    out[col] = f2bf(r);
  }
}

extern "C" void kernel_launch(void* const* d_in, const int* in_sizes, int n_in, void* d_out, int out_size, void* d_ws,
                              size_t ws_size, hipStream_t stream) {
  const unsigned short* x = (const unsigned short*)d_in[0];
  const unsigned short* kc = (const unsigned short*)d_in[1];
  const unsigned short* vc = (const unsigned short*)d_in[2];
  const unsigned short* wq = (const unsigned short*)d_in[3];
  const unsigned short* wo = (const unsigned short*)d_in[4];
  const unsigned short* w1 = (const unsigned short*)d_in[6];
  const unsigned short* b1 = (const unsigned short*)d_in[7];
  const unsigned short* w2 = (const unsigned short*)d_in[8];
  const unsigned short* b2 = (const unsigned short*)d_in[9];
  float* ws = (float*)d_ws;
  float* q = ws;
  float* hh = ws + 8192;
  float* pm = ws + 16384;
  float* pl = ws + 16384 + 256;
  float* po = ws + 32768;
  k1<<<60, 256, 0, stream>>>(x, wq, w1, b1, q, hh);
  k2<<<dim3(NSPLIT, H), 512, 0, stream>>>(kc, vc, q, pm, pl, po);
  k3<<<10, 1024, 0, stream>>>(x, wo, w2, b2, pm, pl, po, hh, (unsigned short*)d_out);
}
```

```cpp
#include <hip/hip_runtime.h>
#include <hip/hip_bf16.h>
#include <stdint.h>

#define PD 4
#define PB1 2
#define STAMPS 0

constexpr int D = 640, H = 32, SK = 8192, DH = 192, F = 1536, QD = H * DH;
constexpr int NSW = 8, NCW = 2, NTH = (NSW + NCW) * 64;
constexpr int NU = 128 / NSW;

using u32x4 = __attribute__((ext_vector_type(4))) unsigned;
using u32x2 = __attribute__((ext_vector_type(2))) unsigned;
using f32x4 = __attribute__((ext_vector_type(4))) float;
using bf16x2 = __attribute__((ext_vector_type(2))) __bf16;
typedef __attribute__((address_space(1))) unsigned long long gu64;
typedef __attribute__((address_space(1))) unsigned gu32;

constexpr size_t WS_EPOCH = 0;
constexpr size_t WS_QP = 4096;
constexpr size_t WS_HP = WS_QP + (size_t)32 * 16 * 192 * 8;
constexpr size_t WS_AP = WS_HP + (size_t)20 * 1536 * 8;
constexpr size_t WS_YP = WS_AP + (size_t)32 * 8 * 256 * 8;
constexpr size_t WS_END = WS_YP + (size_t)8 * 32 * 160 * 8;
constexpr size_t WS_STAMP = (size_t)1 << 26;

__device__ __forceinline__ float lo2f(unsigned u) { return __builtin_bit_cast(float, u << 16); }
__device__ __forceinline__ float hi2f(unsigned u) { return __builtin_bit_cast(float, u & 0xffff0000u); }
__device__ __forceinline__ float rbf(float f) {
  unsigned u = __builtin_bit_cast(unsigned, f);
  u = (u + 0x7FFFu + ((u >> 16) & 1u)) & 0xFFFF0000u;
  return __builtin_bit_cast(float, u);
}
__device__ __forceinline__ float dot2(unsigned k, unsigned q, float acc) {
  return __builtin_amdgcn_fdot2_f32_bf16(__builtin_bit_cast(bf16x2, k), __builtin_bit_cast(bf16x2, q), acc, false);
}
template <int CTRL> __device__ __forceinline__ float dpp(float x) {
  return __builtin_bit_cast(float, __builtin_amdgcn_mov_dpp(__builtin_bit_cast(int, x), CTRL, 0xf, 0xf, true));
}
constexpr int XOR1 = 0xB1, XOR2 = 0x4E, XOR7 = 0x141, XOR8 = 0x128;
__device__ __forceinline__ float swap16_sum(float a, float b) {
  auto s = __builtin_amdgcn_permlane16_swap(__float_as_uint(a), __float_as_uint(b), false, false);
  return __uint_as_float(s[0]) + __uint_as_float(s[1]);
}
__device__ __forceinline__ float swap32_sum(float a, float b) {
  auto s = __builtin_amdgcn_permlane32_swap(__float_as_uint(a), __float_as_uint(b), false, false);
  return __uint_as_float(s[0]) + __uint_as_float(s[1]);
}
__device__ __forceinline__ float swap16_max(float a) {
  auto s = __builtin_amdgcn_permlane16_swap(__float_as_uint(a), __float_as_uint(a), false, false);
  return fmaxf(__uint_as_float(s[0]), __uint_as_float(s[1]));
}
__device__ __forceinline__ float swap32_max(float a) {
  auto s = __builtin_amdgcn_permlane32_swap(__float_as_uint(a), __float_as_uint(a), false, false);
  return fmaxf(__uint_as_float(s[0]), __uint_as_float(s[1]));
}
__device__ __forceinline__ float wave_sum(float v) {
  v += dpp<XOR1>(v); v += dpp<XOR2>(v); v += dpp<XOR7>(v); v += dpp<XOR8>(v);
  v = swap16_sum(v, v); v = swap32_sum(v, v);
  return v;
}
__device__ __forceinline__ float rs8(const float* a, bool b3) {
  float v1[4], v2[2];
#pragma unroll
  for (int j = 0; j < 4; ++j) v1[j] = swap32_sum(a[j], a[j + 4]);
#pragma unroll
  for (int j = 0; j < 2; ++j) v2[j] = swap16_sum(v1[j], v1[j + 2]);
  float t = b3 ? v2[0] : v2[1], keep = b3 ? v2[1] : v2[0];
  return keep + dpp<XOR8>(t);
}
__device__ __forceinline__ void wg_barrier() { asm volatile("s_waitcnt lgkmcnt(0)\n\ts_barrier" ::: "memory"); }
__device__ __forceinline__ void lds_fence() { asm volatile("s_waitcnt lgkmcnt(0)" ::: "memory"); }
__device__ __forceinline__ u32x4 ldnt(const void* p) { return __builtin_nontemporal_load((const u32x4*)p); }
__device__ __forceinline__ void st_gran(void* base, unsigned idx, float v, unsigned tag) {
  __hip_atomic_store((gu64*)base + idx, ((unsigned long long)tag << 32) | __builtin_bit_cast(unsigned, v), __ATOMIC_RELAXED, __HIP_MEMORY_SCOPE_AGENT);
}
__device__ __forceinline__ u32x4 ld_gran2(__amdgpu_buffer_rsrc_t rs, unsigned byte_off) {
  return __builtin_amdgcn_raw_buffer_load_b128(rs, byte_off, 0, 16);
}
constexpr int SPIN_CAP = 1 << 20;

struct __attribute__((aligned(16))) Lds {
  float xn[2][640];
  float yy[2][640];
  unsigned qb[96];
  unsigned tag[4];
  float sm[8], sl[8];
  float sacc[8][192];
  float o[192];
  unsigned wo[192 * 40];
  float red[16][80];
  float m2red[6][80];
  float ymlp[80];
  float hh[48];
  float fin[8][160];
};

__global__ void __launch_bounds__(NTH, 1)
fused_kernel(const unsigned short* __restrict__ x, const u32x4* __restrict__ kc, const u32x4* __restrict__ vc,
             const unsigned short* __restrict__ wq, const unsigned short* __restrict__ wo,
             const unsigned short* __restrict__ w1, const unsigned short* __restrict__ b1,
             const unsigned short* __restrict__ w2, const unsigned short* __restrict__ b2,
             unsigned short* __restrict__ out, unsigned char* __restrict__ ws) {
  __shared__ Lds L;
  const int b = blockIdx.x, xcd = b & 7, jb = b >> 3, h = xcd * 4 + (jb >> 3), s = jb & 7;
  const int tid = threadIdx.x, lane = tid & 63, wave = __builtin_amdgcn_readfirstlane(tid >> 6);
  const int r = lane >> 3, c = lane & 7;
  const bool b3 = (lane & 8) != 0;
  const __amdgpu_buffer_rsrc_t rs = __builtin_amdgcn_make_buffer_rsrc((void*)ws, 0, (int)WS_END, 0x00020000);
#define STAMP(i) do {} while (0)

  if (wave < NSW) {
    const size_t base = ((size_t)h * SK + (size_t)s * 1024) * 24 + r * 24 + c;
    const u32x4* kp = kc + base; const u32x4* vp = vc + base;
    u32x4 kb[PD][3], vb[PD][3];
#define UNIT(i) (wave + NSW * (i))
#define ISSUE(i) do { const int o_ = UNIT(i) * 192; \
    _Pragma("unroll") for (int g = 0; g < 3; ++g) kb[(i) % PD][g] = ldnt(kp + o_ + g * 8); \
    _Pragma("unroll") for (int g = 0; g < 3; ++g) vb[(i) % PD][g] = ldnt(vp + o_ + g * 8); } while (0)
    if (wave == 0) STAMP(0);
#pragma unroll
    for (int i = 0; i < PB1; ++i) ISSUE(i);
    wg_barrier();
#pragma unroll
    for (int i = PB1; i < PD; ++i) ISSUE(i);
    wg_barrier();
    if (wave == 0) STAMP(1);
    unsigned qp[12];
#pragma unroll
    for (int g = 0; g < 3; ++g) { u32x4 t = *(const u32x4*)&L.qb[g * 32 + c * 4]; qp[g * 4] = t[0]; qp[g * 4 + 1] = t[1]; qp[g * 4 + 2] = t[2]; qp[g * 4 + 3] = t[3]; }
    float m = -INFINITY, l = 0.f, acc[24];
#pragma unroll
    for (int i = 0; i < 24; ++i) acc[i] = 0.f;
    const float scale = 0.07216878364870322f;
#pragma unroll
    for (int i = 0; i < NU; ++i) {
      const int sl_ = i % PD;
      float d0 = 0.f, d1 = 0.f, d2 = 0.f;
#pragma unroll
      for (int jj = 0; jj < 4; ++jj) { d0 = dot2(kb[sl_][0][jj], qp[jj], d0); d1 = dot2(kb[sl_][1][jj], qp[4 + jj], d1); d2 = dot2(kb[sl_][2][jj], qp[8 + jj], d2); }
      float d = d0 + d1 + d2;
      d += dpp<XOR1>(d); d += dpp<XOR2>(d); d += dpp<XOR7>(d);
      const float sc = rbf(rbf(d) * scale);
      const float mn = fmaxf(m, sc);
      const float al = __expf(m - mn), p = __expf(sc - mn);
      l = l * al + p; m = mn;
      u32x4 vv[3] = {vb[sl_][0], vb[sl_][1], vb[sl_][2]};
      if (i + PD < NU) ISSUE(i + PD);
#pragma unroll
      for (int g = 0; g < 3; ++g)
#pragma unroll
        for (int jj = 0; jj < 4; ++jj) {
          const int ii = g * 8 + jj * 2;
          acc[ii] = acc[ii] * al + p * lo2f(vv[g][jj]);
          acc[ii + 1] = acc[ii + 1] * al + p * hi2f(vv[g][jj]);
        }
    }
    float M = m;
    M = fmaxf(M, dpp<XOR8>(M)); M = swap16_max(M); M = swap32_max(M);
    const float f = __expf(m - M);
    l *= f;
    l += dpp<XOR8>(l); l = swap16_sum(l, l); l = swap32_sum(l, l);
#pragma unroll
    for (int i = 0; i < 24; ++i) acc[i] *= f;
#pragma unroll
    for (int g = 0; g < 3; ++g) L.sacc[wave][g * 64 + c * 8 + r] = rs8(acc + g * 8, b3);
    if (lane == 0) { L.sm[wave] = M; L.sl[wave] = l; }
    if (wave == 0) STAMP(2);
  } else {
    const int cw = wave - NSW;
    const unsigned ep = *(const unsigned*)(ws + WS_EPOCH);
    unsigned xw[5];
#pragma unroll
    for (int j = 0; j < 5; ++j) xw[j] = ((const unsigned*)x)[lane + 64 * j];
    u32x4 wqv[5][3];
#pragma unroll
    for (int g = 0; g < 5; ++g)
#pragma unroll
      for (int sg = 0; sg < 3; ++sg)
        wqv[g][sg] = ldnt(wq + (size_t)(80 * s + 40 * cw + g * 8 + r) * QD + DH * h + sg * 64 + c * 8);
    const int ks = b / 24, cb = b % 24;
    u32x4 w1v[4];
    if (b < 240) {
#pragma unroll
      for (int i = 0; i < 4; ++i) w1v[i] = ldnt(w1 + (size_t)(ks * 64 + (cw * 4 + i) * 8 + r) * F + cb * 64 + c * 8);
    }
    const unsigned tag = ep + 1u;
    float xv[10];
#pragma unroll
    for (int j = 0; j < 5; ++j) { xv[2 * j] = lo2f(xw[j]); xv[2 * j + 1] = hi2f(xw[j]); }
    {
      float sum = 0.f;
#pragma unroll
      for (int j = 0; j < 10; ++j) sum += xv[j];
      const float mean = wave_sum(sum) * (1.0f / D);
      float s2 = 0.f;
#pragma unroll
      for (int j = 0; j < 10; ++j) { const float d = xv[j] - mean; s2 += d * d; }
      const float var = wave_sum(s2) * (1.0f / D);
      const float mb = rbf(mean), den = rbf(sqrtf(rbf(rbf(var) + 1e-5f)));
#pragma unroll
      for (int j = 0; j < 10; ++j) xv[j] = rbf(rbf(xv[j] - mb) / den);
#pragma unroll
      for (int j = 0; j < 5; ++j) *(float2*)&L.xn[cw][2 * (lane + 64 * j)] = make_float2(xv[2 * j], xv[2 * j + 1]);
    }
    lds_fence();
    {
      float acc[24];
#pragma unroll
      for (int i = 0; i < 24; ++i) acc[i] = 0.f;
#pragma unroll
      for (int g = 0; g < 5; ++g) {
        const float xr = L.xn[cw][80 * s + 40 * cw + g * 8 + r];
#pragma unroll
        for (int sg = 0; sg < 3; ++sg)
#pragma unroll
          for (int jj = 0; jj < 4; ++jj) {
            acc[sg * 8 + 2 * jj] += xr * lo2f(wqv[g][sg][jj]);
            acc[sg * 8 + 2 * jj + 1] += xr * hi2f(wqv[g][sg][jj]);
          }
      }
#pragma unroll
      for (int sg = 0; sg < 3; ++sg)
        st_gran(ws + WS_QP, (unsigned)((h * 16 + s * 2 + cw) * 192 + sg * 64 + c * 8 + r), rs8(acc + sg * 8, b3), tag);
    }
    if (cw == 0) STAMP(3);
    {
      float sum = 0.f;
#pragma unroll
      for (int j = 0; j < 10; ++j) sum += xv[j];
      const float mean = wave_sum(sum) * (1.0f / D);
      float s2 = 0.f;
#pragma unroll
      for (int j = 0; j < 10; ++j) { const float d = xv[j] - mean; s2 += d * d; }
      const float var = wave_sum(s2) * (1.0f / D);
      const float mb = rbf(mean), den = rbf(sqrtf(rbf(rbf(var) + 1e-5f)));
#pragma unroll
      for (int j = 0; j < 5; ++j)
        *(float2*)&L.yy[cw][2 * (lane + 64 * j)] = make_float2(rbf(rbf(xv[2 * j] - mb) / den), rbf(rbf(xv[2 * j + 1] - mb) / den));
      lds_fence();
      if (b < 240) {
        float acc[8];
#pragma unroll
        for (int i = 0; i < 8; ++i) acc[i] = 0.f;
#pragma unroll
        for (int i = 0; i < 4; ++i) {
          const float yr = L.yy[cw][ks * 64 + (cw * 4 + i) * 8 + r];
#pragma unroll
          for (int jj = 0; jj < 4; ++jj) { acc[2 * jj] += yr * lo2f(w1v[i][jj]); acc[2 * jj + 1] += yr * hi2f(w1v[i][jj]); }
        }
        st_gran(ws + WS_HP, (unsigned)((ks * 2 + cw) * 1536 + cb * 64 + c * 8 + r), rs8(acc, b3), tag);
      }
    }
    {
      const unsigned off0 = (unsigned)(WS_QP + (size_t)(h * 16) * 192 * 8) + (unsigned)(cw * 48 + (lane < 48 ? lane : 47)) * 16u;
      u32x4 g[16];
#pragma unroll
      for (int p = 0; p < 16; ++p) g[p] = ld_gran2(rs, off0 + p * 1536u);
      asm volatile("s_barrier" ::: "memory");
      for (int spin = 0; spin < SPIN_CAP; ++spin) {
        asm volatile("" ::: "memory");
        bool ok = true;
#pragma unroll
        for (int p = 0; p < 16; ++p) ok &= (g[p][1] == tag) & (g[p][3] == tag);
        if (__all(ok)) break;
        __builtin_amdgcn_s_sleep(2);
#pragma unroll
        for (int p = 0; p < 16; ++p) g[p] = ld_gran2(rs, off0 + p * 1536u);
      }
      float q0 = 0.f, q1 = 0.f;
#pragma unroll
      for (int p = 0; p < 16; ++p) { q0 += __uint_as_float(g[p][0]); q1 += __uint_as_float(g[p][2]); }
      if (lane < 48) L.qb[cw * 48 + lane] = (__builtin_bit_cast(unsigned, rbf(q0)) >> 16) | __builtin_bit_cast(unsigned, rbf(q1));
      if (lane == 0 && cw == 0) L.tag[0] = tag;
    }
    wg_barrier();
    if (cw == 0) STAMP(4);
    {
      u32x4 t[15];
#pragma unroll
      for (int i = 0; i < 15; ++i) {
        const int idx = cw * 960 + i * 64 + lane, row = idx / 10, ch = idx % 10;
        t[i] = ldnt(wo + (size_t)(DH * h + row) * D + 80 * s + ch * 8);
      }
      const int r6 = lane / 10, c10 = lane % 10;
      u32x4 w2v[8];
      if (cw == 1) {
#pragma unroll
        for (int i = 0; i < 8; ++i) w2v[i] = ldnt(w2 + (size_t)(48 * h + i * 6 + (lane < 60 ? r6 : 0)) * D + 80 * s + c10 * 8);
      }
#pragma unroll
      for (int i = 0; i < 15; ++i) *(u32x4*)&L.wo[(cw * 960 + i * 64 + lane) * 4] = t[i];
      if (cw == 1) {
        const int pr = lane % 24, pg = lane / 24;
        const unsigned off0 = (unsigned)WS_HP + (unsigned)((lane < 48 ? pg : 0) * 10 * 1536 + 48 * h + 2 * pr) * 8u;
        u32x4 g[10];
        for (int spin = 0; spin < SPIN_CAP; ++spin) {
        asm volatile("" ::: "memory");
          bool ok = true;
#pragma unroll
          for (int p = 0; p < 10; ++p) g[p] = ld_gran2(rs, off0 + p * 1536u * 8u);
#pragma unroll
          for (int p = 0; p < 10; ++p) ok &= (g[p][1] == tag) & (g[p][3] == tag);
          if (__all(ok)) break;
          __builtin_amdgcn_s_sleep(8);
        }
        float a0 = 0.f, a1 = 0.f;
#pragma unroll
        for (int p = 0; p < 10; ++p) { a0 += __uint_as_float(g[p][0]); a1 += __uint_as_float(g[p][2]); }
        const float o0 = __shfl(a0, lane + 24), o1 = __shfl(a1, lane + 24);
        if (lane < 24) {
          const unsigned bb = ((const unsigned*)b1)[24 * h + lane];
          float t0 = rbf(rbf(a0 + o0) + lo2f(bb)), t1 = rbf(rbf(a1 + o1) + hi2f(bb));
          float g0 = 0.5f * t0 * (1.0f + tanhf(0.7978845608028654f * (t0 + 0.044715f * t0 * t0 * t0)));
          float g1 = 0.5f * t1 * (1.0f + tanhf(0.7978845608028654f * (t1 + 0.044715f * t1 * t1 * t1)));
          *(float2*)&L.hh[2 * lane] = make_float2(rbf(g0), rbf(g1));
        }
        lds_fence();
        float acc[8];
#pragma unroll
        for (int i = 0; i < 8; ++i) acc[i] = 0.f;
#pragma unroll
        for (int i = 0; i < 8; ++i) {
          const float hv = L.hh[i * 6 + (lane < 60 ? r6 : 0)];
#pragma unroll
          for (int jj = 0; jj < 4; ++jj) { acc[2 * jj] += hv * lo2f(w2v[i][jj]); acc[2 * jj + 1] += hv * hi2f(w2v[i][jj]); }
        }
        if (lane < 60) {
          *(f32x4*)&L.m2red[r6][c10 * 8] = f32x4{acc[0], acc[1], acc[2], acc[3]};
          *(f32x4*)&L.m2red[r6][c10 * 8 + 4] = f32x4{acc[4], acc[5], acc[6], acc[7]};
        }
        lds_fence();
#pragma unroll
        for (int k = 0; k < 2; ++k) {
          const int col = lane + 64 * k;
          if (col < 80) L.ymlp[col] = L.m2red[0][col] + L.m2red[1][col] + L.m2red[2][col] + L.m2red[3][col] + L.m2red[4][col] + L.m2red[5][col];
        }
      }
    }
  }
  wg_barrier();
  const unsigned tag = L.tag[0];
  if (tid < 192) {
    float MM = L.sm[0];
#pragma unroll
    for (int i = 1; i < 8; ++i) MM = fmaxf(MM, L.sm[i]);
    float o = 0.f, ll = 0.f;
#pragma unroll
    for (int i = 0; i < 8; ++i) { const float e = __expf(L.sm[i] - MM); o += e * L.sacc[i][tid]; ll += e * L.sl[i]; }
    void* ap = ws + WS_AP + (size_t)(h * 8 + s) * 256 * 8;
    st_gran(ap, tid, o, tag);
    if (tid == 0) { st_gran(ap, 192, MM, tag); st_gran(ap, 193, ll, tag); }
  }
  if (tid == 0) STAMP(5);
  if (wave < 2) {
    const int pr = tid < 96 ? tid : 95;
    const unsigned off0 = (unsigned)(WS_AP + (size_t)(h * 8) * 256 * 8);
    u32x4 g[8], ml[8];
    for (int spin = 0; spin < SPIN_CAP; ++spin) {
        asm volatile("" ::: "memory");
      bool ok = true;
#pragma unroll
      for (int p = 0; p < 8; ++p) { g[p] = ld_gran2(rs, off0 + p * 2048u + pr * 16u); ml[p] = ld_gran2(rs, off0 + p * 2048u + 96 * 16u); }
#pragma unroll
      for (int p = 0; p < 8; ++p) ok &= (g[p][1] == tag) & (g[p][3] == tag) & (ml[p][1] == tag) & (ml[p][3] == tag);
      if (__all(ok)) break;
      __builtin_amdgcn_s_sleep(1);
    }
    float MM = __uint_as_float(ml[0][0]);
#pragma unroll
    for (int p = 1; p < 8; ++p) MM = fmaxf(MM, __uint_as_float(ml[p][0]));
    float o0 = 0.f, o1 = 0.f, ll = 0.f;
#pragma unroll
    for (int p = 0; p < 8; ++p) {
      const float e = __expf(__uint_as_float(ml[p][0]) - MM);
      o0 += e * __uint_as_float(g[p][0]); o1 += e * __uint_as_float(g[p][2]); ll += e * __uint_as_float(ml[p][2]);
    }
    const float inv = 1.0f / ll;
    if (tid < 96) *(float2*)&L.o[2 * tid] = make_float2(rbf(o0 * inv), rbf(o1 * inv));
  }
  wg_barrier();
  if (tid == 0) STAMP(6);
  {
    const int cp = tid % 40, rg = tid / 40;
    float a0 = 0.f, a1 = 0.f;
#pragma unroll
    for (int i = 0; i < 12; ++i) {
      const float ov = L.o[rg * 12 + i];
      const unsigned w = L.wo[(rg * 12 + i) * 40 + cp];
      a0 += ov * lo2f(w); a1 += ov * hi2f(w);
    }
    *(float2*)&L.red[rg][2 * cp] = make_float2(a0, a1);
  }
  wg_barrier();
  if (tid < 160) {
    float v;
    if (tid < 80) {
      v = 0.f;
#pragma unroll
      for (int i = 0; i < 16; ++i) v += L.red[i][tid];
    } else v = L.ymlp[tid - 80];
    st_gran(ws + WS_YP + (size_t)(s * 32 + h) * 160 * 8, tid, v, tag);
  }
  if (tid == 0) STAMP(7);
  if (h != 0) return;
  {
    const int pr = tid % 80, pg = tid / 80;
    const unsigned off0 = (unsigned)(WS_YP + (size_t)(s * 32 + pg * 4) * 160 * 8) + pr * 16u;
    u32x4 g[4];
    for (int spin = 0; spin < SPIN_CAP; ++spin) {
        asm volatile("" ::: "memory");
      bool ok = true;
#pragma unroll
      for (int p = 0; p < 4; ++p) g[p] = ld_gran2(rs, off0 + p * 1280u);
#pragma unroll
      for (int p = 0; p < 4; ++p) ok &= (g[p][1] == tag) & (g[p][3] == tag);
      if (__all(ok)) break;
      __builtin_amdgcn_s_sleep(1);
    }
    float a0 = 0.f, a1 = 0.f;
#pragma unroll
    for (int p = 0; p < 4; ++p) { a0 += __uint_as_float(g[p][0]); a1 += __uint_as_float(g[p][2]); }
    *(float2*)&L.fin[pg][2 * pr] = make_float2(a0, a1);
  }
  wg_barrier();
  if (tid < 80) {
    float sa = 0.f, t = 0.f;
#pragma unroll
    for (int i = 0; i < 8; ++i) { sa += L.fin[i][tid]; t += L.fin[i][80 + tid]; }
    const int col = 80 * s + tid;
    sa = rbf(sa);
    t = rbf(rbf(t) + lo2f(b2[col]));
    const float g = rbf(t / (1.0f + __expf(-t)));
    const float res = rbf(lo2f(x[col]) + rbf(sa * g));
    out[col] = (unsigned short)(__builtin_bit_cast(unsigned, res) >> 16);
    if (tid == 0 && s == 0) *(unsigned*)(ws + WS_EPOCH) = tag;
  }
  if (tid == 0) STAMP(8);
}

extern "C" void kernel_launch(void* const* d_in, const int* in_sizes, int n_in, void* d_out, int out_size, void* d_ws,
                              size_t ws_size, hipStream_t stream) {
  fused_kernel<<<256, NTH, 0, stream>>>((const unsigned short*)d_in[0], (const u32x4*)d_in[1], (const u32x4*)d_in[2],
                                         (const unsigned short*)d_in[3], (const unsigned short*)d_in[4],
                                         (const unsigned short*)d_in[6], (const unsigned short*)d_in[7],
                                         (const unsigned short*)d_in[8], (const unsigned short*)d_in[9],
                                         (unsigned short*)d_out, (unsigned char*)d_ws);
}
```

```cpp
#include <hip/hip_runtime.h>
#include <hip/hip_bf16.h>
#include <stdint.h>

#define PD 4
#define PB1 2
#define STAMPS 0

constexpr int D = 640, H = 32, SK = 8192, DH = 192, F = 1536, QD = H * DH;
constexpr int NSW = 8, NCW = 2, NTH = (NSW + NCW) * 64;
constexpr int NU = 128 / NSW;

using u32x4 = __attribute__((ext_vector_type(4))) unsigned;
using u32x2 = __attribute__((ext_vector_type(2))) unsigned;
using f32x4 = __attribute__((ext_vector_type(4))) float;
using bf16x2 = __attribute__((ext_vector_type(2))) __bf16;
typedef __attribute__((address_space(1))) unsigned long long gu64;
typedef __attribute__((address_space(1))) unsigned gu32;

constexpr size_t WS_EPOCH = 0;
constexpr size_t WS_QP = 4096;
constexpr size_t WS_HP = WS_QP + (size_t)32 * 16 * 192 * 8;
constexpr size_t WS_AP = WS_HP + (size_t)20 * 1536 * 8;
constexpr size_t WS_YP = WS_AP + (size_t)32 * 8 * 256 * 8;
constexpr size_t WS_END = WS_YP + (size_t)8 * 32 * 160 * 8;
constexpr size_t WS_STAMP = (size_t)1 << 26;

__device__ __forceinline__ float lo2f(unsigned u) { return __builtin_bit_cast(float, u << 16); }
__device__ __forceinline__ float hi2f(unsigned u) { return __builtin_bit_cast(float, u & 0xffff0000u); }
__device__ __forceinline__ float rbf(float f) {
  unsigned u = __builtin_bit_cast(unsigned, f);
  u = (u + 0x7FFFu + ((u >> 16) & 1u)) & 0xFFFF0000u;
  return __builtin_bit_cast(float, u);
}
__device__ __forceinline__ float dot2(unsigned k, unsigned q, float acc) {
  return __builtin_amdgcn_fdot2_f32_bf16(__builtin_bit_cast(bf16x2, k), __builtin_bit_cast(bf16x2, q), acc, false);
}
template <int CTRL> __device__ __forceinline__ float dpp(float x) {
  return __builtin_bit_cast(float, __builtin_amdgcn_mov_dpp(__builtin_bit_cast(int, x), CTRL, 0xf, 0xf, true));
}
constexpr int XOR1 = 0xB1, XOR2 = 0x4E, XOR7 = 0x141, XOR8 = 0x128;
__device__ __forceinline__ float swap16_sum(float a, float b) {
  auto s = __builtin_amdgcn_permlane16_swap(__float_as_uint(a), __float_as_uint(b), false, false);
  return __uint_as_float(s[0]) + __uint_as_float(s[1]);
}
__device__ __forceinline__ float swap32_sum(float a, float b) {
  auto s = __builtin_amdgcn_permlane32_swap(__float_as_uint(a), __float_as_uint(b), false, false);
  return __uint_as_float(s[0]) + __uint_as_float(s[1]);
}
__device__ __forceinline__ float swap16_max(float a) {
  auto s = __builtin_amdgcn_permlane16_swap(__float_as_uint(a), __float_as_uint(a), false, false);
  return fmaxf(__uint_as_float(s[0]), __uint_as_float(s[1]));
}
__device__ __forceinline__ float swap32_max(float a) {
  auto s = __builtin_amdgcn_permlane32_swap(__float_as_uint(a), __float_as_uint(a), false, false);
  return fmaxf(__uint_as_float(s[0]), __uint_as_float(s[1]));
}
__device__ __forceinline__ float wave_sum(float v) {
  v += dpp<XOR1>(v); v += dpp<XOR2>(v); v += dpp<XOR7>(v); v += dpp<XOR8>(v);
  v = swap16_sum(v, v); v = swap32_sum(v, v);
  return v;
}
__device__ __forceinline__ float rs8(const float* a, bool b3) {
  float v1[4], v2[2];
#pragma unroll
  for (int j = 0; j < 4; ++j) v1[j] = swap32_sum(a[j], a[j + 4]);
#pragma unroll
  for (int j = 0; j < 2; ++j) v2[j] = swap16_sum(v1[j], v1[j + 2]);
  float t = b3 ? v2[0] : v2[1], keep = b3 ? v2[1] : v2[0];
  return keep + dpp<XOR8>(t);
}
__device__ __forceinline__ void wg_barrier() { asm volatile("s_waitcnt lgkmcnt(0)\n\ts_barrier" ::: "memory"); }
__device__ __forceinline__ void lds_fence() { asm volatile("s_waitcnt lgkmcnt(0)" ::: "memory"); }
__device__ __forceinline__ u32x4 ldnt(const void* p) { return __builtin_nontemporal_load((const u32x4*)p); }
__device__ __forceinline__ void st_gran(void* base, unsigned idx, float v, unsigned tag) {
  __hip_atomic_store((gu64*)base + idx, ((unsigned long long)tag << 32) | __builtin_bit_cast(unsigned, v), __ATOMIC_RELAXED, __HIP_MEMORY_SCOPE_AGENT);
}
__device__ __forceinline__ u32x4 ld_gran2(__amdgpu_buffer_rsrc_t rs, unsigned byte_off) {
  return __builtin_amdgcn_raw_buffer_load_b128(rs, byte_off, 0, 16);
}
constexpr int SPIN_CAP = 1 << 20;

struct __attribute__((aligned(16))) Lds {
  float xn[2][640];
  float yy[2][640];
  unsigned qb[96];
  unsigned tag[4];
  float sm[8], sl[8];
  float sacc[8][192];
  float o[192];
  unsigned wo[192 * 40];
  float red[16][80];
  float m2red[6][80];
  float ymlp[80];
  float hh[48];
  float fin[8][160];
};

__global__ void __launch_bounds__(NTH, 1)
fused_kernel(const unsigned short* __restrict__ x, const u32x4* __restrict__ kc, const u32x4* __restrict__ vc,
             const unsigned short* __restrict__ wq, const unsigned short* __restrict__ wo,
             const unsigned short* __restrict__ w1, const unsigned short* __restrict__ b1,
             const unsigned short* __restrict__ w2, const unsigned short* __restrict__ b2,
             unsigned short* __restrict__ out, unsigned char* __restrict__ ws) {
  __shared__ Lds L;
  const int b = blockIdx.x, xcd = b & 7, jb = b >> 3, h = xcd * 4 + (jb >> 3), s = jb & 7;
  const int tid = threadIdx.x, lane = tid & 63, wave = __builtin_amdgcn_readfirstlane(tid >> 6);
  const int r = lane >> 3, c = lane & 7;
  const bool b3 = (lane & 8) != 0;
  const __amdgpu_buffer_rsrc_t rs = __builtin_amdgcn_make_buffer_rsrc((void*)ws, 0, (int)WS_END, 0x00020000);
#define STAMP(i) do {} while (0)

  if (wave >= NCW) {
    const int sw = wave - NCW;
    const size_t base = ((size_t)h * SK + (size_t)s * 1024) * 24 + r * 24 + c;
    const u32x4* kp = kc + base; const u32x4* vp = vc + base;
    u32x4 kb[PD][3], vb[PD][3];
#define UNIT(i) (sw + NSW * (i))
#define ISSUE(i) do { const int o_ = UNIT(i) * 192; \
    _Pragma("unroll") for (int g = 0; g < 3; ++g) kb[(i) % PD][g] = ldnt(kp + o_ + g * 8); \
    _Pragma("unroll") for (int g = 0; g < 3; ++g) vb[(i) % PD][g] = ldnt(vp + o_ + g * 8); } while (0)
    if (sw == 0) STAMP(0);
#pragma unroll
    for (int i = 0; i < PB1; ++i) ISSUE(i);
    wg_barrier();
#pragma unroll
    for (int i = PB1; i < PD; ++i) ISSUE(i);
    if (sw == 0) STAMP(1);
    unsigned qp[12];
#pragma unroll
    for (int g = 0; g < 3; ++g) { u32x4 t = *(const u32x4*)&L.qb[g * 32 + c * 4]; qp[g * 4] = t[0]; qp[g * 4 + 1] = t[1]; qp[g * 4 + 2] = t[2]; qp[g * 4 + 3] = t[3]; }
    float m = -INFINITY, l = 0.f, acc[24];
#pragma unroll
    for (int i = 0; i < 24; ++i) acc[i] = 0.f;
    const float scale = 0.07216878364870322f;
#pragma unroll
    for (int i = 0; i < NU; ++i) {
      const int sl_ = i % PD;
      float d0 = 0.f, d1 = 0.f, d2 = 0.f;
#pragma unroll
      for (int jj = 0; jj < 4; ++jj) { d0 = dot2(kb[sl_][0][jj], qp[jj], d0); d1 = dot2(kb[sl_][1][jj], qp[4 + jj], d1); d2 = dot2(kb[sl_][2][jj], qp[8 + jj], d2); }
      float d = d0 + d1 + d2;
      d += dpp<XOR1>(d); d += dpp<XOR2>(d); d += dpp<XOR7>(d);
      const float sc = rbf(rbf(d) * scale);
      const float mn = fmaxf(m, sc);
      const float al = __expf(m - mn), p = __expf(sc - mn);
      l = l * al + p; m = mn;
      u32x4 vv[3] = {vb[sl_][0], vb[sl_][1], vb[sl_][2]};
      if (i + PD < NU) ISSUE(i + PD);
#pragma unroll
      for (int g = 0; g < 3; ++g)
#pragma unroll
        for (int jj = 0; jj < 4; ++jj) {
          const int ii = g * 8 + jj * 2;
          acc[ii] = acc[ii] * al + p * lo2f(vv[g][jj]);
          acc[ii + 1] = acc[ii + 1] * al + p * hi2f(vv[g][jj]);
        }
    }
    float M = m;
    M = fmaxf(M, dpp<XOR8>(M)); M = swap16_max(M); M = swap32_max(M);
    const float f = __expf(m - M);
    l *= f;
    l += dpp<XOR8>(l); l = swap16_sum(l, l); l = swap32_sum(l, l);
#pragma unroll
    for (int i = 0; i < 24; ++i) acc[i] *= f;
#pragma unroll
    for (int g = 0; g < 3; ++g) L.sacc[sw][g * 64 + c * 8 + r] = rs8(acc + g * 8, b3);
    if (lane == 0) { L.sm[sw] = M; L.sl[sw] = l; }
    if (sw == 0) STAMP(2);
    if (sw == 7) STAMP(9);
  } else {
    const int cw = wave;
    const unsigned ep = *(const unsigned*)(ws + WS_EPOCH);
    unsigned xw[5];
#pragma unroll
    for (int j = 0; j < 5; ++j) xw[j] = ((const unsigned*)x)[lane + 64 * j];
    u32x4 wqv[5][3];
#pragma unroll
    for (int g = 0; g < 5; ++g)
#pragma unroll
      for (int sg = 0; sg < 3; ++sg)
        wqv[g][sg] = ldnt(wq + (size_t)(80 * s + 40 * cw + g * 8 + r) * QD + DH * h + sg * 64 + c * 8);
    const int ks = b / 24, cb = b % 24;
    u32x4 w1v[4];
    if (b < 240) {
#pragma unroll
      for (int i = 0; i < 4; ++i) w1v[i] = ldnt(w1 + (size_t)(ks * 64 + (cw * 4 + i) * 8 + r) * F + cb * 64 + c * 8);
    }
    const unsigned tag = ep + 1u;
    float xv[10];
#pragma unroll
    for (int j = 0; j < 5; ++j) { xv[2 * j] = lo2f(xw[j]); xv[2 * j + 1] = hi2f(xw[j]); }
    {
      float sum = 0.f;
#pragma unroll
      for (int j = 0; j < 10; ++j) sum += xv[j];
      const float mean = wave_sum(sum) * (1.0f / D);
      float s2 = 0.f;
#pragma unroll
      for (int j = 0; j < 10; ++j) { const float d = xv[j] - mean; s2 += d * d; }
      const float var = wave_sum(s2) * (1.0f / D);
      const float mb = rbf(mean), den = rbf(sqrtf(rbf(rbf(var) + 1e-5f)));
#pragma unroll
      for (int j = 0; j < 10; ++j) xv[j] = rbf(rbf(xv[j] - mb) / den);
#pragma unroll
      for (int j = 0; j < 5; ++j) *(float2*)&L.xn[cw][2 * (lane + 64 * j)] = make_float2(xv[2 * j], xv[2 * j + 1]);
    }
    lds_fence();
    {
      float acc[24];
#pragma unroll
      for (int i = 0; i < 24; ++i) acc[i] = 0.f;
#pragma unroll
      for (int g = 0; g < 5; ++g) {
        const float xr = L.xn[cw][80 * s + 40 * cw + g * 8 + r];
#pragma unroll
        for (int sg = 0; sg < 3; ++sg)
#pragma unroll
          for (int jj = 0; jj < 4; ++jj) {
            acc[sg * 8 + 2 * jj] += xr * lo2f(wqv[g][sg][jj]);
            acc[sg * 8 + 2 * jj + 1] += xr * hi2f(wqv[g][sg][jj]);
          }
      }
#pragma unroll
      for (int sg = 0; sg < 3; ++sg)
        st_gran(ws + WS_QP, (unsigned)((h * 16 + s * 2 + cw) * 192 + sg * 64 + c * 8 + r), rs8(acc + sg * 8, b3), tag);
    }
    if (cw == 0) STAMP(3);
    {
      float sum = 0.f;
#pragma unroll
      for (int j = 0; j < 10; ++j) sum += xv[j];
      const float mean = wave_sum(sum) * (1.0f / D);
      float s2 = 0.f;
#pragma unroll
      for (int j = 0; j < 10; ++j) { const float d = xv[j] - mean; s2 += d * d; }
      const float var = wave_sum(s2) * (1.0f / D);
      const float mb = rbf(mean), den = rbf(sqrtf(rbf(rbf(var) + 1e-5f)));
#pragma unroll
      for (int j = 0; j < 5; ++j)
        *(float2*)&L.yy[cw][2 * (lane + 64 * j)] = make_float2(rbf(rbf(xv[2 * j] - mb) / den), rbf(rbf(xv[2 * j + 1] - mb) / den));
      lds_fence();
      if (b < 240) {
        float acc[8];
#pragma unroll
        for (int i = 0; i < 8; ++i) acc[i] = 0.f;
#pragma unroll
        for (int i = 0; i < 4; ++i) {
          const float yr = L.yy[cw][ks * 64 + (cw * 4 + i) * 8 + r];
#pragma unroll
          for (int jj = 0; jj < 4; ++jj) { acc[2 * jj] += yr * lo2f(w1v[i][jj]); acc[2 * jj + 1] += yr * hi2f(w1v[i][jj]); }
        }
        st_gran(ws + WS_HP, (unsigned)((ks * 2 + cw) * 1536 + cb * 64 + c * 8 + r), rs8(acc, b3), tag);
      }
    }
    {
      const unsigned off0 = (unsigned)(WS_QP + (size_t)(h * 16) * 192 * 8) + (unsigned)(cw * 48 + (lane < 48 ? lane : 47)) * 16u;
      u32x4 g[16];
#pragma unroll
      for (int p = 0; p < 16; ++p) g[p] = ld_gran2(rs, off0 + p * 1536u);
      for (int spin = 0; spin < SPIN_CAP; ++spin) {
        asm volatile("" ::: "memory");
        bool ok = true;
#pragma unroll
        for (int p = 0; p < 16; ++p) ok &= (g[p][1] == tag) & (g[p][3] == tag);
        if (__all(ok)) break;
        __builtin_amdgcn_s_sleep(1);
#pragma unroll
        for (int p = 0; p < 16; ++p) g[p] = ld_gran2(rs, off0 + p * 1536u);
      }
      float q0 = 0.f, q1 = 0.f;
#pragma unroll
      for (int p = 0; p < 16; ++p) { q0 += __uint_as_float(g[p][0]); q1 += __uint_as_float(g[p][2]); }
      if (lane < 48) L.qb[cw * 48 + lane] = (__builtin_bit_cast(unsigned, rbf(q0)) >> 16) | __builtin_bit_cast(unsigned, rbf(q1));
      if (lane == 0 && cw == 0) L.tag[0] = tag;
    }
    wg_barrier();
    if (cw == 0) STAMP(4);
    {
      u32x4 t[15];
#pragma unroll
      for (int i = 0; i < 15; ++i) {
        const int idx = cw * 960 + i * 64 + lane, row = idx / 10, ch = idx % 10;
        t[i] = ldnt(wo + (size_t)(DH * h + row) * D + 80 * s + ch * 8);
      }
      const int r6 = lane / 10, c10 = lane % 10;
      u32x4 w2v[8];
      if (cw == 1) {
#pragma unroll
        for (int i = 0; i < 8; ++i) w2v[i] = ldnt(w2 + (size_t)(48 * h + i * 6 + (lane < 60 ? r6 : 0)) * D + 80 * s + c10 * 8);
      }
#pragma unroll
      for (int i = 0; i < 15; ++i) *(u32x4*)&L.wo[(cw * 960 + i * 64 + lane) * 4] = t[i];
      if (cw == 1) {
        const int pr = lane % 24, pg = lane / 24;
        const unsigned off0 = (unsigned)WS_HP + (unsigned)((lane < 48 ? pg : 0) * 10 * 1536 + 48 * h + 2 * pr) * 8u;
        u32x4 g[10];
        for (int spin = 0; spin < SPIN_CAP; ++spin) {
        asm volatile("" ::: "memory");
          bool ok = true;
#pragma unroll
          for (int p = 0; p < 10; ++p) g[p] = ld_gran2(rs, off0 + p * 1536u * 8u);
#pragma unroll
          for (int p = 0; p < 10; ++p) ok &= (g[p][1] == tag) & (g[p][3] == tag);
          if (__all(ok)) break;
          __builtin_amdgcn_s_sleep(8);
        }
        float a0 = 0.f, a1 = 0.f;
#pragma unroll
        for (int p = 0; p < 10; ++p) { a0 += __uint_as_float(g[p][0]); a1 += __uint_as_float(g[p][2]); }
        const float o0 = __shfl(a0, lane + 24), o1 = __shfl(a1, lane + 24);
        if (lane < 24) {
          const unsigned bb = ((const unsigned*)b1)[24 * h + lane];
          float t0 = rbf(rbf(a0 + o0) + lo2f(bb)), t1 = rbf(rbf(a1 + o1) + hi2f(bb));
          float g0 = 0.5f * t0 * (1.0f + tanhf(0.7978845608028654f * (t0 + 0.044715f * t0 * t0 * t0)));
          float g1 = 0.5f * t1 * (1.0f + tanhf(0.7978845608028654f * (t1 + 0.044715f * t1 * t1 * t1)));
          *(float2*)&L.hh[2 * lane] = make_float2(rbf(g0), rbf(g1));
        }
        lds_fence();
        float acc[8];
#pragma unroll
        for (int i = 0; i < 8; ++i) acc[i] = 0.f;
#pragma unroll
        for (int i = 0; i < 8; ++i) {
          const float hv = L.hh[i * 6 + (lane < 60 ? r6 : 0)];
#pragma unroll
          for (int jj = 0; jj < 4; ++jj) { acc[2 * jj] += hv * lo2f(w2v[i][jj]); acc[2 * jj + 1] += hv * hi2f(w2v[i][jj]); }
        }
        if (lane < 60) {
          *(f32x4*)&L.m2red[r6][c10 * 8] = f32x4{acc[0], acc[1], acc[2], acc[3]};
          *(f32x4*)&L.m2red[r6][c10 * 8 + 4] = f32x4{acc[4], acc[5], acc[6], acc[7]};
        }
        lds_fence();
#pragma unroll
        for (int k = 0; k < 2; ++k) {
          const int col = lane + 64 * k;
          if (col < 80) L.ymlp[col] = L.m2red[0][col] + L.m2red[1][col] + L.m2red[2][col] + L.m2red[3][col] + L.m2red[4][col] + L.m2red[5][col];
        }
      }
    }
  }
  wg_barrier();
  const unsigned tag = L.tag[0];
  if (tid < 192) {
    float MM = L.sm[0];
#pragma unroll
    for (int i = 1; i < 8; ++i) MM = fmaxf(MM, L.sm[i]);
    float o = 0.f, ll = 0.f;
#pragma unroll
    for (int i = 0; i < 8; ++i) { const float e = __expf(L.sm[i] - MM); o += e * L.sacc[i][tid]; ll += e * L.sl[i]; }
    void* ap = ws + WS_AP + (size_t)(h * 8 + s) * 256 * 8;
    st_gran(ap, tid, o, tag);
    if (tid == 0) { st_gran(ap, 192, MM, tag); st_gran(ap, 193, ll, tag); }
  }
  if (tid == 0) STAMP(5);
  if (wave < 2) {
    const int pr = tid < 96 ? tid : 95;
    const unsigned off0 = (unsigned)(WS_AP + (size_t)(h * 8) * 256 * 8);
    u32x4 g[8], ml[8];
    for (int spin = 0; spin < SPIN_CAP; ++spin) {
        asm volatile("" ::: "memory");
      bool ok = true;
#pragma unroll
      for (int p = 0; p < 8; ++p) { g[p] = ld_gran2(rs, off0 + p * 2048u + pr * 16u); ml[p] = ld_gran2(rs, off0 + p * 2048u + 96 * 16u); }
#pragma unroll
      for (int p = 0; p < 8; ++p) ok &= (g[p][1] == tag) & (g[p][3] == tag) & (ml[p][1] == tag) & (ml[p][3] == tag);
      if (__all(ok)) break;
      __builtin_amdgcn_s_sleep(1);
    }
    float MM = __uint_as_float(ml[0][0]);
#pragma unroll
    for (int p = 1; p < 8; ++p) MM = fmaxf(MM, __uint_as_float(ml[p][0]));
    float o0 = 0.f, o1 = 0.f, ll = 0.f;
#pragma unroll
    for (int p = 0; p < 8; ++p) {
      const float e = __expf(__uint_as_float(ml[p][0]) - MM);
      o0 += e * __uint_as_float(g[p][0]); o1 += e * __uint_as_float(g[p][2]); ll += e * __uint_as_float(ml[p][2]);
    }
    const float inv = 1.0f / ll;
    if (tid < 96) *(float2*)&L.o[2 * tid] = make_float2(rbf(o0 * inv), rbf(o1 * inv));
  }
  wg_barrier();
  if (tid == 0) STAMP(6);
  {
    const int cp = tid % 40, rg = tid / 40;
    float a0 = 0.f, a1 = 0.f;
#pragma unroll
    for (int i = 0; i < 12; ++i) {
      const float ov = L.o[rg * 12 + i];
      const unsigned w = L.wo[(rg * 12 + i) * 40 + cp];
      a0 += ov * lo2f(w); a1 += ov * hi2f(w);
    }
    *(float2*)&L.red[rg][2 * cp] = make_float2(a0, a1);
  }
  wg_barrier();
  if (tid < 160) {
    float v;
    if (tid < 80) {
      v = 0.f;
#pragma unroll
      for (int i = 0; i < 16; ++i) v += L.red[i][tid];
    } else v = L.ymlp[tid - 80];
    st_gran(ws + WS_YP + (size_t)(s * 32 + h) * 160 * 8, tid, v, tag);
  }
  if (tid == 0) STAMP(7);
  if (h != 0) return;
  {
    const int pr = tid % 80, pg = tid / 80;
    const unsigned off0 = (unsigned)(WS_YP + (size_t)(s * 32 + pg * 4) * 160 * 8) + pr * 16u;
    u32x4 g[4];
    for (int spin = 0; spin < SPIN_CAP; ++spin) {
        asm volatile("" ::: "memory");
      bool ok = true;
#pragma unroll
      for (int p = 0; p < 4; ++p) g[p] = ld_gran2(rs, off0 + p * 1280u);
#pragma unroll
      for (int p = 0; p < 4; ++p) ok &= (g[p][1] == tag) & (g[p][3] == tag);
      if (__all(ok)) break;
      __builtin_amdgcn_s_sleep(1);
    }
    float a0 = 0.f, a1 = 0.f;
#pragma unroll
    for (int p = 0; p < 4; ++p) { a0 += __uint_as_float(g[p][0]); a1 += __uint_as_float(g[p][2]); }
    *(float2*)&L.fin[pg][2 * pr] = make_float2(a0, a1);
  }
  wg_barrier();
  if (tid < 80) {
    float sa = 0.f, t = 0.f;
#pragma unroll
    for (int i = 0; i < 8; ++i) { sa += L.fin[i][tid]; t += L.fin[i][80 + tid]; }
    const int col = 80 * s + tid;
    sa = rbf(sa);
    t = rbf(rbf(t) + lo2f(b2[col]));
    const float g = rbf(t / (1.0f + __expf(-t)));
    const float res = rbf(lo2f(x[col]) + rbf(sa * g));
    out[col] = (unsigned short)(__builtin_bit_cast(unsigned, res) >> 16);
    if (tid == 0 && s == 0) *(unsigned*)(ws + WS_EPOCH) = tag;
  }
  if (tid == 0) STAMP(8);
}

extern "C" void kernel_launch(void* const* d_in, const int* in_sizes, int n_in, void* d_out, int out_size, void* d_ws,
                              size_t ws_size, hipStream_t stream) {
  fused_kernel<<<256, NTH, 0, stream>>>((const unsigned short*)d_in[0], (const u32x4*)d_in[1], (const u32x4*)d_in[2],
                                         (const unsigned short*)d_in[3], (const unsigned short*)d_in[4],
                                         (const unsigned short*)d_in[6], (const unsigned short*)d_in[7],
                                         (const unsigned short*)d_in[8], (const unsigned short*)d_in[9],
                                         (unsigned short*)d_out, (unsigned char*)d_ws);
}
```

```cpp
#include <hip/hip_runtime.h>
#include <hip/hip_bf16.h>
#include <stdint.h>

#define PD 4
#define PB1 1
#define PACE 1
#define STAMPS 0

constexpr int D = 640, H = 32, SK = 8192, DH = 192, F = 1536, QD = H * DH;
constexpr int NSW = 8, NCW = 2, NTH = (NSW + NCW) * 64;
constexpr int NU = 128 / NSW;

using u32x4 = __attribute__((ext_vector_type(4))) unsigned;
using u32x2 = __attribute__((ext_vector_type(2))) unsigned;
using f32x4 = __attribute__((ext_vector_type(4))) float;
using bf16x2 = __attribute__((ext_vector_type(2))) __bf16;
typedef __attribute__((address_space(1))) unsigned long long gu64;
typedef __attribute__((address_space(1))) unsigned gu32;

constexpr size_t WS_EPOCH = 0;
constexpr size_t WS_QP = 4096;
constexpr size_t WS_HP = WS_QP + (size_t)32 * 16 * 192 * 8;
constexpr size_t WS_AP = WS_HP + (size_t)20 * 1536 * 8;
constexpr size_t WS_YP = WS_AP + (size_t)32 * 8 * 256 * 8;
constexpr size_t WS_END = WS_YP + (size_t)8 * 32 * 160 * 8;
constexpr size_t WS_STAMP = (size_t)1 << 26;

__device__ __forceinline__ float lo2f(unsigned u) { return __builtin_bit_cast(float, u << 16); }
__device__ __forceinline__ float hi2f(unsigned u) { return __builtin_bit_cast(float, u & 0xffff0000u); }
__device__ __forceinline__ float rbf(float f) {
  unsigned u = __builtin_bit_cast(unsigned, f);
  u = (u + 0x7FFFu + ((u >> 16) & 1u)) & 0xFFFF0000u;
  return __builtin_bit_cast(float, u);
}
__device__ __forceinline__ float dot2(unsigned k, unsigned q, float acc) {
  return __builtin_amdgcn_fdot2_f32_bf16(__builtin_bit_cast(bf16x2, k), __builtin_bit_cast(bf16x2, q), acc, false);
}
template <int CTRL> __device__ __forceinline__ float dpp(float x) {
  return __builtin_bit_cast(float, __builtin_amdgcn_mov_dpp(__builtin_bit_cast(int, x), CTRL, 0xf, 0xf, true));
}
constexpr int XOR1 = 0xB1, XOR2 = 0x4E, XOR7 = 0x141, XOR8 = 0x128;
__device__ __forceinline__ float swap16_sum(float a, float b) {
  auto s = __builtin_amdgcn_permlane16_swap(__float_as_uint(a), __float_as_uint(b), false, false);
  return __uint_as_float(s[0]) + __uint_as_float(s[1]);
}
__device__ __forceinline__ float swap32_sum(float a, float b) {
  auto s = __builtin_amdgcn_permlane32_swap(__float_as_uint(a), __float_as_uint(b), false, false);
  return __uint_as_float(s[0]) + __uint_as_float(s[1]);
}
__device__ __forceinline__ float swap16_max(float a) {
  auto s = __builtin_amdgcn_permlane16_swap(__float_as_uint(a), __float_as_uint(a), false, false);
  return fmaxf(__uint_as_float(s[0]), __uint_as_float(s[1]));
}
__device__ __forceinline__ float swap32_max(float a) {
  auto s = __builtin_amdgcn_permlane32_swap(__float_as_uint(a), __float_as_uint(a), false, false);
  return fmaxf(__uint_as_float(s[0]), __uint_as_float(s[1]));
}
__device__ __forceinline__ float wave_sum(float v) {
  v += dpp<XOR1>(v); v += dpp<XOR2>(v); v += dpp<XOR7>(v); v += dpp<XOR8>(v);
  v = swap16_sum(v, v); v = swap32_sum(v, v);
  return v;
}
__device__ __forceinline__ float rs8(const float* a, bool b3) {
  float v1[4], v2[2];
#pragma unroll
  for (int j = 0; j < 4; ++j) v1[j] = swap32_sum(a[j], a[j + 4]);
#pragma unroll
  for (int j = 0; j < 2; ++j) v2[j] = swap16_sum(v1[j], v1[j + 2]);
  float t = b3 ? v2[0] : v2[1], keep = b3 ? v2[1] : v2[0];
  return keep + dpp<XOR8>(t);
}
__device__ __forceinline__ void wg_barrier() { asm volatile("s_waitcnt lgkmcnt(0)\n\ts_barrier" ::: "memory"); }
__device__ __forceinline__ void lds_fence() { asm volatile("s_waitcnt lgkmcnt(0)" ::: "memory"); }
__device__ __forceinline__ u32x4 ldnt(const void* p) { return __builtin_nontemporal_load((const u32x4*)p); }
__device__ __forceinline__ void st_gran(void* base, unsigned idx, float v, unsigned tag) {
  __hip_atomic_store((gu64*)base + idx, ((unsigned long long)tag << 32) | __builtin_bit_cast(unsigned, v), __ATOMIC_RELAXED, __HIP_MEMORY_SCOPE_AGENT);
}
__device__ __forceinline__ u32x4 ld_gran2(__amdgpu_buffer_rsrc_t rs, unsigned byte_off) {
  return __builtin_amdgcn_raw_buffer_load_b128(rs, byte_off, 0, 16);
}
constexpr int SPIN_CAP = 1 << 20;

struct __attribute__((aligned(16))) Lds {
  float xn[2][640];
  float yy[2][640];
  unsigned qb[96];
  unsigned tag[4];
  float sm[8], sl[8];
  float sacc[8][192];
  float o[192];
  unsigned wo[192 * 40];
  float red[16][80];
  float m2red[6][80];
  float ymlp[80];
  float hh[48];
  float fin[8][160];
};

__global__ void __launch_bounds__(NTH, 1)
fused_kernel(const unsigned short* __restrict__ x, const u32x4* __restrict__ kc, const u32x4* __restrict__ vc,
             const unsigned short* __restrict__ wq, const unsigned short* __restrict__ wo,
             const unsigned short* __restrict__ w1, const unsigned short* __restrict__ b1,
             const unsigned short* __restrict__ w2, const unsigned short* __restrict__ b2,
             unsigned short* __restrict__ out, unsigned char* __restrict__ ws) {
  __shared__ Lds L;
  const int b = blockIdx.x, xcd = b & 7, jb = b >> 3, h = xcd * 4 + (jb >> 3), s = jb & 7;
  const int tid = threadIdx.x, lane = tid & 63, wave = __builtin_amdgcn_readfirstlane(tid >> 6);
  const int r = lane >> 3, c = lane & 7;
  const bool b3 = (lane & 8) != 0;
  const __amdgpu_buffer_rsrc_t rs = __builtin_amdgcn_make_buffer_rsrc((void*)ws, 0, (int)WS_END, 0x00020000);
#define STAMP(i) do {} while (0)

  if (wave >= NCW) {
    const int sw = wave - NCW;
    const size_t base = ((size_t)h * SK + (size_t)s * 1024) * 24 + r * 24 + c;
    const u32x4* kp = kc + base; const u32x4* vp = vc + base;
    u32x4 kb[PD][3], vb[PD][3];
#define UNIT(i) (sw + NSW * (i))
#define ISSUE(i) do { const int o_ = UNIT(i) * 192; \
    _Pragma("unroll") for (int g = 0; g < 3; ++g) kb[(i) % PD][g] = ldnt(kp + o_ + g * 8); \
    _Pragma("unroll") for (int g = 0; g < 3; ++g) vb[(i) % PD][g] = ldnt(vp + o_ + g * 8); } while (0)
    if (sw == 0) STAMP(0);
#pragma unroll
    for (int i = 0; i < PB1; ++i) ISSUE(i);
#pragma unroll
    for (int i = PB1; i < PD; ++i) {
      asm volatile("s_waitcnt vmcnt(%0)" :: "n"(6 * (PB1 - 1)) : "memory");
      ISSUE(i);
    }
    wg_barrier();
    if (sw == 0) STAMP(1);
    unsigned qp[12];
#pragma unroll
    for (int g = 0; g < 3; ++g) { u32x4 t = *(const u32x4*)&L.qb[g * 32 + c * 4]; qp[g * 4] = t[0]; qp[g * 4 + 1] = t[1]; qp[g * 4 + 2] = t[2]; qp[g * 4 + 3] = t[3]; }
    float m = -INFINITY, l = 0.f, acc[24];
#pragma unroll
    for (int i = 0; i < 24; ++i) acc[i] = 0.f;
    const float scale = 0.07216878364870322f;
#pragma unroll
    for (int i = 0; i < NU; ++i) {
      const int sl_ = i % PD;
      float d0 = 0.f, d1 = 0.f, d2 = 0.f;
#pragma unroll
      for (int jj = 0; jj < 4; ++jj) { d0 = dot2(kb[sl_][0][jj], qp[jj], d0); d1 = dot2(kb[sl_][1][jj], qp[4 + jj], d1); d2 = dot2(kb[sl_][2][jj], qp[8 + jj], d2); }
      float d = d0 + d1 + d2;
      d += dpp<XOR1>(d); d += dpp<XOR2>(d); d += dpp<XOR7>(d);
      const float sc = rbf(rbf(d) * scale);
      const float mn = fmaxf(m, sc);
      const float al = __expf(m - mn), p = __expf(sc - mn);
      l = l * al + p; m = mn;
      u32x4 vv[3] = {vb[sl_][0], vb[sl_][1], vb[sl_][2]};
      if (i + PD < NU) ISSUE(i + PD);
#pragma unroll
      for (int g = 0; g < 3; ++g)
#pragma unroll
        for (int jj = 0; jj < 4; ++jj) {
          const int ii = g * 8 + jj * 2;
          acc[ii] = acc[ii] * al + p * lo2f(vv[g][jj]);
          acc[ii + 1] = acc[ii + 1] * al + p * hi2f(vv[g][jj]);
        }
    }
    float M = m;
    M = fmaxf(M, dpp<XOR8>(M)); M = swap16_max(M); M = swap32_max(M);
    const float f = __expf(m - M);
    l *= f;
    l += dpp<XOR8>(l); l = swap16_sum(l, l); l = swap32_sum(l, l);
#pragma unroll
    for (int i = 0; i < 24; ++i) acc[i] *= f;
#pragma unroll
    for (int g = 0; g < 3; ++g) L.sacc[sw][g * 64 + c * 8 + r] = rs8(acc + g * 8, b3);
    if (lane == 0) { L.sm[sw] = M; L.sl[sw] = l; }
    if (sw == 0) STAMP(2);
    if (sw == 7) STAMP(9);
  } else {
    const int cw = wave;
    const unsigned ep = *(const unsigned*)(ws + WS_EPOCH);
    unsigned xw[5];
#pragma unroll
    for (int j = 0; j < 5; ++j) xw[j] = ((const unsigned*)x)[lane + 64 * j];
    u32x4 wqv[5][3];
#pragma unroll
    for (int g = 0; g < 5; ++g)
#pragma unroll
      for (int sg = 0; sg < 3; ++sg)
        wqv[g][sg] = ldnt(wq + (size_t)(80 * s + 40 * cw + g * 8 + r) * QD + DH * h + sg * 64 + c * 8);
    const int ks = b / 24, cb = b % 24;
    u32x4 w1v[4];
    if (b < 240) {
#pragma unroll
      for (int i = 0; i < 4; ++i) w1v[i] = ldnt(w1 + (size_t)(ks * 64 + (cw * 4 + i) * 8 + r) * F + cb * 64 + c * 8);
    }
    const unsigned tag = ep + 1u;
    float xv[10];
#pragma unroll
    for (int j = 0; j < 5; ++j) { xv[2 * j] = lo2f(xw[j]); xv[2 * j + 1] = hi2f(xw[j]); }
    {
      float sum = 0.f;
#pragma unroll
      for (int j = 0; j < 10; ++j) sum += xv[j];
      const float mean = wave_sum(sum) * (1.0f / D);
      float s2 = 0.f;
#pragma unroll
      for (int j = 0; j < 10; ++j) { const float d = xv[j] - mean; s2 += d * d; }
      const float var = wave_sum(s2) * (1.0f / D);
      const float mb = rbf(mean), den = rbf(sqrtf(rbf(rbf(var) + 1e-5f)));
#pragma unroll
      for (int j = 0; j < 10; ++j) xv[j] = rbf(rbf(xv[j] - mb) / den);
#pragma unroll
      for (int j = 0; j < 5; ++j) *(float2*)&L.xn[cw][2 * (lane + 64 * j)] = make_float2(xv[2 * j], xv[2 * j + 1]);
    }
    lds_fence();
    {
      float acc[24];
#pragma unroll
      for (int i = 0; i < 24; ++i) acc[i] = 0.f;
#pragma unroll
      for (int g = 0; g < 5; ++g) {
        const float xr = L.xn[cw][80 * s + 40 * cw + g * 8 + r];
#pragma unroll
        for (int sg = 0; sg < 3; ++sg)
#pragma unroll
          for (int jj = 0; jj < 4; ++jj) {
            acc[sg * 8 + 2 * jj] += xr * lo2f(wqv[g][sg][jj]);
            acc[sg * 8 + 2 * jj + 1] += xr * hi2f(wqv[g][sg][jj]);
          }
      }
#pragma unroll
      for (int sg = 0; sg < 3; ++sg)
        st_gran(ws + WS_QP, (unsigned)((h * 16 + s * 2 + cw) * 192 + sg * 64 + c * 8 + r), rs8(acc + sg * 8, b3), tag);
    }
    if (cw == 0) STAMP(3);
    {
      float sum = 0.f;
#pragma unroll
      for (int j = 0; j < 10; ++j) sum += xv[j];
      const float mean = wave_sum(sum) * (1.0f / D);
      float s2 = 0.f;
#pragma unroll
      for (int j = 0; j < 10; ++j) { const float d = xv[j] - mean; s2 += d * d; }
      const float var = wave_sum(s2) * (1.0f / D);
      const float mb = rbf(mean), den = rbf(sqrtf(rbf(rbf(var) + 1e-5f)));
#pragma unroll
      for (int j = 0; j < 5; ++j)
        *(float2*)&L.yy[cw][2 * (lane + 64 * j)] = make_float2(rbf(rbf(xv[2 * j] - mb) / den), rbf(rbf(xv[2 * j + 1] - mb) / den));
      lds_fence();
      if (b < 240) {
        float acc[8];
#pragma unroll
        for (int i = 0; i < 8; ++i) acc[i] = 0.f;
#pragma unroll
        for (int i = 0; i < 4; ++i) {
          const float yr = L.yy[cw][ks * 64 + (cw * 4 + i) * 8 + r];
#pragma unroll
          for (int jj = 0; jj < 4; ++jj) { acc[2 * jj] += yr * lo2f(w1v[i][jj]); acc[2 * jj + 1] += yr * hi2f(w1v[i][jj]); }
        }
        st_gran(ws + WS_HP, (unsigned)((ks * 2 + cw) * 1536 + cb * 64 + c * 8 + r), rs8(acc, b3), tag);
      }
    }
    {
      const unsigned off0 = (unsigned)(WS_QP + (size_t)(h * 16) * 192 * 8) + (unsigned)(cw * 48 + (lane < 48 ? lane : 47)) * 16u;
      u32x4 g[16];
#pragma unroll
      for (int p = 0; p < 16; ++p) g[p] = ld_gran2(rs, off0 + p * 1536u);
      for (int spin = 0; spin < SPIN_CAP; ++spin) {
        asm volatile("" ::: "memory");
        bool ok = true;
#pragma unroll
        for (int p = 0; p < 16; ++p) ok &= (g[p][1] == tag) & (g[p][3] == tag);
        if (__all(ok)) break;
        __builtin_amdgcn_s_sleep(1);
#pragma unroll
        for (int p = 0; p < 16; ++p) g[p] = ld_gran2(rs, off0 + p * 1536u);
      }
      float q0 = 0.f, q1 = 0.f;
#pragma unroll
      for (int p = 0; p < 16; ++p) { q0 += __uint_as_float(g[p][0]); q1 += __uint_as_float(g[p][2]); }
      if (lane < 48) L.qb[cw * 48 + lane] = (__builtin_bit_cast(unsigned, rbf(q0)) >> 16) | __builtin_bit_cast(unsigned, rbf(q1));
      if (lane == 0 && cw == 0) L.tag[0] = tag;
    }
    wg_barrier();
    if (cw == 0) STAMP(4);
    {
      u32x4 t[15];
#pragma unroll
      for (int i = 0; i < 15; ++i) {
        const int idx = cw * 960 + i * 64 + lane, row = idx / 10, ch = idx % 10;
        t[i] = ldnt(wo + (size_t)(DH * h + row) * D + 80 * s + ch * 8);
      }
      const int r6 = lane / 10, c10 = lane % 10;
      u32x4 w2v[8];
      if (cw == 1) {
#pragma unroll
        for (int i = 0; i < 8; ++i) w2v[i] = ldnt(w2 + (size_t)(48 * h + i * 6 + (lane < 60 ? r6 : 0)) * D + 80 * s + c10 * 8);
      }
#pragma unroll
      for (int i = 0; i < 15; ++i) *(u32x4*)&L.wo[(cw * 960 + i * 64 + lane) * 4] = t[i];
      if (cw == 1) {
        const int pr = lane % 24, pg = lane / 24;
        const unsigned off0 = (unsigned)WS_HP + (unsigned)((lane < 48 ? pg : 0) * 10 * 1536 + 48 * h + 2 * pr) * 8u;
        u32x4 g[10];
        for (int spin = 0; spin < SPIN_CAP; ++spin) {
        asm volatile("" ::: "memory");
          bool ok = true;
#pragma unroll
          for (int p = 0; p < 10; ++p) g[p] = ld_gran2(rs, off0 + p * 1536u * 8u);
#pragma unroll
          for (int p = 0; p < 10; ++p) ok &= (g[p][1] == tag) & (g[p][3] == tag);
          if (__all(ok)) break;
          __builtin_amdgcn_s_sleep(8);
        }
        float a0 = 0.f, a1 = 0.f;
#pragma unroll
        for (int p = 0; p < 10; ++p) { a0 += __uint_as_float(g[p][0]); a1 += __uint_as_float(g[p][2]); }
        const float o0 = __shfl(a0, lane + 24), o1 = __shfl(a1, lane + 24);
        if (lane < 24) {
          const unsigned bb = ((const unsigned*)b1)[24 * h + lane];
          float t0 = rbf(rbf(a0 + o0) + lo2f(bb)), t1 = rbf(rbf(a1 + o1) + hi2f(bb));
          float g0 = 0.5f * t0 * (1.0f + tanhf(0.7978845608028654f * (t0 + 0.044715f * t0 * t0 * t0)));
          float g1 = 0.5f * t1 * (1.0f + tanhf(0.7978845608028654f * (t1 + 0.044715f * t1 * t1 * t1)));
          *(float2*)&L.hh[2 * lane] = make_float2(rbf(g0), rbf(g1));
        }
        lds_fence();
        float acc[8];
#pragma unroll
        for (int i = 0; i < 8; ++i) acc[i] = 0.f;
#pragma unroll
        for (int i = 0; i < 8; ++i) {
          const float hv = L.hh[i * 6 + (lane < 60 ? r6 : 0)];
#pragma unroll
          for (int jj = 0; jj < 4; ++jj) { acc[2 * jj] += hv * lo2f(w2v[i][jj]); acc[2 * jj + 1] += hv * hi2f(w2v[i][jj]); }
        }
        if (lane < 60) {
          *(f32x4*)&L.m2red[r6][c10 * 8] = f32x4{acc[0], acc[1], acc[2], acc[3]};
          *(f32x4*)&L.m2red[r6][c10 * 8 + 4] = f32x4{acc[4], acc[5], acc[6], acc[7]};
        }
        lds_fence();
#pragma unroll
        for (int k = 0; k < 2; ++k) {
          const int col = lane + 64 * k;
          if (col < 80) L.ymlp[col] = L.m2red[0][col] + L.m2red[1][col] + L.m2red[2][col] + L.m2red[3][col] + L.m2red[4][col] + L.m2red[5][col];
        }
      }
    }
  }
  wg_barrier();
  const unsigned tag = L.tag[0];
  if (tid < 192) {
    float MM = L.sm[0];
#pragma unroll
    for (int i = 1; i < 8; ++i) MM = fmaxf(MM, L.sm[i]);
    float o = 0.f, ll = 0.f;
#pragma unroll
    for (int i = 0; i < 8; ++i) { const float e = __expf(L.sm[i] - MM); o += e * L.sacc[i][tid]; ll += e * L.sl[i]; }
    void* ap = ws + WS_AP + (size_t)(h * 8 + s) * 256 * 8;
    st_gran(ap, tid, o, tag);
    if (tid == 0) { st_gran(ap, 192, MM, tag); st_gran(ap, 193, ll, tag); }
  }
  if (tid == 0) STAMP(5);
  if (wave < 2) {
    const int pr = tid < 96 ? tid : 95;
    const unsigned off0 = (unsigned)(WS_AP + (size_t)(h * 8) * 256 * 8);
    u32x4 g[8], ml[8];
    for (int spin = 0; spin < SPIN_CAP; ++spin) {
        asm volatile("" ::: "memory");
      bool ok = true;
#pragma unroll
      for (int p = 0; p < 8; ++p) { g[p] = ld_gran2(rs, off0 + p * 2048u + pr * 16u); ml[p] = ld_gran2(rs, off0 + p * 2048u + 96 * 16u); }
#pragma unroll
      for (int p = 0; p < 8; ++p) ok &= (g[p][1] == tag) & (g[p][3] == tag) & (ml[p][1] == tag) & (ml[p][3] == tag);
      if (__all(ok)) break;
      __builtin_amdgcn_s_sleep(1);
    }
    float MM = __uint_as_float(ml[0][0]);
#pragma unroll
    for (int p = 1; p < 8; ++p) MM = fmaxf(MM, __uint_as_float(ml[p][0]));
    float o0 = 0.f, o1 = 0.f, ll = 0.f;
#pragma unroll
    for (int p = 0; p < 8; ++p) {
      const float e = __expf(__uint_as_float(ml[p][0]) - MM);
      o0 += e * __uint_as_float(g[p][0]); o1 += e * __uint_as_float(g[p][2]); ll += e * __uint_as_float(ml[p][2]);
    }
    const float inv = 1.0f / ll;
    if (tid < 96) *(float2*)&L.o[2 * tid] = make_float2(rbf(o0 * inv), rbf(o1 * inv));
  }
  wg_barrier();
  if (tid == 0) STAMP(6);
  {
    const int cp = tid % 40, rg = tid / 40;
    float a0 = 0.f, a1 = 0.f;
#pragma unroll
    for (int i = 0; i < 12; ++i) {
      const float ov = L.o[rg * 12 + i];
      const unsigned w = L.wo[(rg * 12 + i) * 40 + cp];
      a0 += ov * lo2f(w); a1 += ov * hi2f(w);
    }
    *(float2*)&L.red[rg][2 * cp] = make_float2(a0, a1);
  }
  wg_barrier();
  if (tid < 160) {
    float v;
    if (tid < 80) {
      v = 0.f;
#pragma unroll
      for (int i = 0; i < 16; ++i) v += L.red[i][tid];
    } else v = L.ymlp[tid - 80];
    st_gran(ws + WS_YP + (size_t)(s * 32 + h) * 160 * 8, tid, v, tag);
  }
  if (tid == 0) STAMP(7);
  if (h != 0) return;
  {
    const int pr = tid % 80, pg = tid / 80;
    const unsigned off0 = (unsigned)(WS_YP + (size_t)(s * 32 + pg * 4) * 160 * 8) + pr * 16u;
    u32x4 g[4];
    for (int spin = 0; spin < SPIN_CAP; ++spin) {
        asm volatile("" ::: "memory");
      bool ok = true;
#pragma unroll
      for (int p = 0; p < 4; ++p) g[p] = ld_gran2(rs, off0 + p * 1280u);
#pragma unroll
      for (int p = 0; p < 4; ++p) ok &= (g[p][1] == tag) & (g[p][3] == tag);
      if (__all(ok)) break;
      __builtin_amdgcn_s_sleep(1);
    }
    float a0 = 0.f, a1 = 0.f;
#pragma unroll
    for (int p = 0; p < 4; ++p) { a0 += __uint_as_float(g[p][0]); a1 += __uint_as_float(g[p][2]); }
    *(float2*)&L.fin[pg][2 * pr] = make_float2(a0, a1);
  }
  wg_barrier();
  if (tid < 80) {
    float sa = 0.f, t = 0.f;
#pragma unroll
    for (int i = 0; i < 8; ++i) { sa += L.fin[i][tid]; t += L.fin[i][80 + tid]; }
    const int col = 80 * s + tid;
    sa = rbf(sa);
    t = rbf(rbf(t) + lo2f(b2[col]));
    const float g = rbf(t / (1.0f + __expf(-t)));
    const float res = rbf(lo2f(x[col]) + rbf(sa * g));
    out[col] = (unsigned short)(__builtin_bit_cast(unsigned, res) >> 16);
    if (tid == 0 && s == 0) *(unsigned*)(ws + WS_EPOCH) = tag;
  }
  if (tid == 0) STAMP(8);
}

extern "C" void kernel_launch(void* const* d_in, const int* in_sizes, int n_in, void* d_out, int out_size, void* d_ws,
                              size_t ws_size, hipStream_t stream) {
  fused_kernel<<<256, NTH, 0, stream>>>((const unsigned short*)d_in[0], (const u32x4*)d_in[1], (const u32x4*)d_in[2],
                                         (const unsigned short*)d_in[3], (const unsigned short*)d_in[4],
                                         (const unsigned short*)d_in[6], (const unsigned short*)d_in[7],
                                         (const unsigned short*)d_in[8], (const unsigned short*)d_in[9],
                                         (unsigned short*)d_out, (unsigned char*)d_ws);
}
```

```cpp
#include <hip/hip_runtime.h>
#include <hip/hip_bf16.h>
#include <stdint.h>

#define PD 4
#define PB1 1
#define PACE 1
#define KVAUX 2
#define STAMPS 0

constexpr int D = 640, H = 32, SK = 8192, DH = 192, F = 1536, QD = H * DH;
constexpr int NSW = 8, NCW = 2, NTH = (NSW + NCW) * 64;
constexpr int NU = 128 / NSW;

using u32x4 = __attribute__((ext_vector_type(4))) unsigned;
using u32x2 = __attribute__((ext_vector_type(2))) unsigned;
using f32x4 = __attribute__((ext_vector_type(4))) float;
using bf16x2 = __attribute__((ext_vector_type(2))) __bf16;
typedef __attribute__((address_space(1))) unsigned long long gu64;
typedef __attribute__((address_space(1))) unsigned gu32;

constexpr size_t WS_EPOCH = 0;
constexpr size_t WS_QP = 4096;
constexpr size_t WS_HP = WS_QP + (size_t)32 * 16 * 192 * 8;
constexpr size_t WS_AP = WS_HP + (size_t)20 * 1536 * 8;
constexpr size_t WS_YP = WS_AP + (size_t)32 * 8 * 256 * 8;
constexpr size_t WS_END = WS_YP + (size_t)8 * 32 * 160 * 8;
constexpr size_t WS_STAMP = (size_t)1 << 26;

__device__ __forceinline__ float lo2f(unsigned u) { return __builtin_bit_cast(float, u << 16); }
__device__ __forceinline__ float hi2f(unsigned u) { return __builtin_bit_cast(float, u & 0xffff0000u); }
__device__ __forceinline__ float rbf(float f) {
  unsigned u = __builtin_bit_cast(unsigned, f);
  u = (u + 0x7FFFu + ((u >> 16) & 1u)) & 0xFFFF0000u;
  return __builtin_bit_cast(float, u);
}
__device__ __forceinline__ float dot2(unsigned k, unsigned q, float acc) {
  return __builtin_amdgcn_fdot2_f32_bf16(__builtin_bit_cast(bf16x2, k), __builtin_bit_cast(bf16x2, q), acc, false);
}
template <int CTRL> __device__ __forceinline__ float dpp(float x) {
  return __builtin_bit_cast(float, __builtin_amdgcn_mov_dpp(__builtin_bit_cast(int, x), CTRL, 0xf, 0xf, true));
}
constexpr int XOR1 = 0xB1, XOR2 = 0x4E, XOR7 = 0x141, XOR8 = 0x128;
__device__ __forceinline__ float swap16_sum(float a, float b) {
  auto s = __builtin_amdgcn_permlane16_swap(__float_as_uint(a), __float_as_uint(b), false, false);
  return __uint_as_float(s[0]) + __uint_as_float(s[1]);
}
__device__ __forceinline__ float swap32_sum(float a, float b) {
  auto s = __builtin_amdgcn_permlane32_swap(__float_as_uint(a), __float_as_uint(b), false, false);
  return __uint_as_float(s[0]) + __uint_as_float(s[1]);
}
__device__ __forceinline__ float swap16_max(float a) {
  auto s = __builtin_amdgcn_permlane16_swap(__float_as_uint(a), __float_as_uint(a), false, false);
  return fmaxf(__uint_as_float(s[0]), __uint_as_float(s[1]));
}
__device__ __forceinline__ float swap32_max(float a) {
  auto s = __builtin_amdgcn_permlane32_swap(__float_as_uint(a), __float_as_uint(a), false, false);
  return fmaxf(__uint_as_float(s[0]), __uint_as_float(s[1]));
}
__device__ __forceinline__ float wave_sum(float v) {
  v += dpp<XOR1>(v); v += dpp<XOR2>(v); v += dpp<XOR7>(v); v += dpp<XOR8>(v);
  v = swap16_sum(v, v); v = swap32_sum(v, v);
  return v;
}
__device__ __forceinline__ float rs8(const float* a, bool b3) {
  float v1[4], v2[2];
#pragma unroll
  for (int j = 0; j < 4; ++j) v1[j] = swap32_sum(a[j], a[j + 4]);
#pragma unroll
  for (int j = 0; j < 2; ++j) v2[j] = swap16_sum(v1[j], v1[j + 2]);
  float t = b3 ? v2[0] : v2[1], keep = b3 ? v2[1] : v2[0];
  return keep + dpp<XOR8>(t);
}
__device__ __forceinline__ void wg_barrier() { asm volatile("s_waitcnt lgkmcnt(0)\n\ts_barrier" ::: "memory"); }
__device__ __forceinline__ void lds_fence() { asm volatile("s_waitcnt lgkmcnt(0)" ::: "memory"); }
__device__ __forceinline__ u32x4 ldnt(const void* p) { return __builtin_nontemporal_load((const u32x4*)p); }
__device__ __forceinline__ void st_gran(void* base, unsigned idx, float v, unsigned tag) {
  __hip_atomic_store((gu64*)base + idx, ((unsigned long long)tag << 32) | __builtin_bit_cast(unsigned, v), __ATOMIC_RELAXED, __HIP_MEMORY_SCOPE_AGENT);
}
__device__ __forceinline__ u32x4 ld_gran2(__amdgpu_buffer_rsrc_t rs, unsigned byte_off) {
  return __builtin_amdgcn_raw_buffer_load_b128(rs, byte_off, 0, 16);
}
constexpr int SPIN_CAP = 1 << 20;

struct __attribute__((aligned(16))) Lds {
  float xn[2][640];
  float yy[2][640];
  unsigned qb[96];
  unsigned tag[4];
  float sm[8], sl[8];
  float sacc[8][192];
  float o[192];
  unsigned wo[192 * 40];
  float red[16][80];
  float m2red[6][80];
  float ymlp[80];
  float hh[48];
  float fin[8][160];
};

__global__ void __launch_bounds__(NTH, 1)
fused_kernel(const unsigned short* __restrict__ x, const u32x4* __restrict__ kc, const u32x4* __restrict__ vc,
             const unsigned short* __restrict__ wq, const unsigned short* __restrict__ wo,
             const unsigned short* __restrict__ w1, const unsigned short* __restrict__ b1,
             const unsigned short* __restrict__ w2, const unsigned short* __restrict__ b2,
             unsigned short* __restrict__ out, unsigned char* __restrict__ ws) {
  __shared__ Lds L;
  const int b = blockIdx.x, xcd = b & 7, jb = b >> 3, h = xcd * 4 + (jb >> 3), s = jb & 7;
  const int tid = threadIdx.x, lane = tid & 63, wave = __builtin_amdgcn_readfirstlane(tid >> 6);
  const int r = lane >> 3, c = lane & 7;
  const bool b3 = (lane & 8) != 0;
  const __amdgpu_buffer_rsrc_t rs = __builtin_amdgcn_make_buffer_rsrc((void*)ws, 0, (int)WS_END, 0x00020000);
#define STAMP(i) do {} while (0)

  if (wave >= NCW) {
    const int sw = wave - NCW;
    const size_t base = ((size_t)h * SK + (size_t)s * 1024) * 24;
    const __amdgpu_buffer_rsrc_t rk = __builtin_amdgcn_make_buffer_rsrc((void*)(kc + base), 0, 1024 * 384, 0x00020000);
    const __amdgpu_buffer_rsrc_t rv = __builtin_amdgcn_make_buffer_rsrc((void*)(vc + base), 0, 1024 * 384, 0x00020000);
    const unsigned voff = (unsigned)(r * 24 + c) * 16u;
    u32x4 kb[PD][3], vb[PD][3];
#define UNIT(i) (sw + NSW * (i))
#define ISSUE2(slot, u) do { const unsigned so_ = (unsigned)(u) * 3072u; \
    _Pragma("unroll") for (int g = 0; g < 3; ++g) kb[slot][g] = __builtin_amdgcn_raw_buffer_load_b128(rk, voff + g * 128, so_, KVAUX); \
    _Pragma("unroll") for (int g = 0; g < 3; ++g) vb[slot][g] = __builtin_amdgcn_raw_buffer_load_b128(rv, voff + g * 128, so_, KVAUX); } while (0)
#define ISSUE(i) ISSUE2((i) % PD, UNIT(i))
    if (sw == 0) STAMP(0);
#pragma unroll
    for (int i = 0; i < PB1; ++i) ISSUE(i);
#pragma unroll
    for (int i = PB1; i < PD; ++i) {
      asm volatile("s_waitcnt vmcnt(%0)" :: "n"(6 * (PB1 - 1)) : "memory");
      ISSUE(i);
    }
    wg_barrier();
    if (sw == 0) STAMP(1);
    unsigned qp[12];
#pragma unroll
    for (int g = 0; g < 3; ++g) { u32x4 t = *(const u32x4*)&L.qb[g * 32 + c * 4]; qp[g * 4] = t[0]; qp[g * 4 + 1] = t[1]; qp[g * 4 + 2] = t[2]; qp[g * 4 + 3] = t[3]; }
    float m = -INFINITY, l = 0.f, acc[24];
#pragma unroll
    for (int i = 0; i < 24; ++i) acc[i] = 0.f;
    const float scale = 0.07216878364870322f;
#pragma unroll
    for (int i = 0; i < NU; ++i) {
      const int sl_ = i % PD;
      float d0 = 0.f, d1 = 0.f, d2 = 0.f;
#pragma unroll
      for (int jj = 0; jj < 4; ++jj) { d0 = dot2(kb[sl_][0][jj], qp[jj], d0); d1 = dot2(kb[sl_][1][jj], qp[4 + jj], d1); d2 = dot2(kb[sl_][2][jj], qp[8 + jj], d2); }
      float d = d0 + d1 + d2;
      d += dpp<XOR1>(d); d += dpp<XOR2>(d); d += dpp<XOR7>(d);
      const float sc = rbf(rbf(d) * scale);
      const float mn = fmaxf(m, sc);
      const float al = __expf(m - mn), p = __expf(sc - mn);
      l = l * al + p; m = mn;
      u32x4 vv[3] = {vb[sl_][0], vb[sl_][1], vb[sl_][2]};
      if (i + PD < NU) ISSUE(i + PD);
#pragma unroll
      for (int g = 0; g < 3; ++g)
#pragma unroll
        for (int jj = 0; jj < 4; ++jj) {
          const int ii = g * 8 + jj * 2;
          acc[ii] = acc[ii] * al + p * lo2f(vv[g][jj]);
          acc[ii + 1] = acc[ii + 1] * al + p * hi2f(vv[g][jj]);
        }
    }
    float M = m;
    M = fmaxf(M, dpp<XOR8>(M)); M = swap16_max(M); M = swap32_max(M);
    const float f = __expf(m - M);
    l *= f;
    l += dpp<XOR8>(l); l = swap16_sum(l, l); l = swap32_sum(l, l);
#pragma unroll
    for (int i = 0; i < 24; ++i) acc[i] *= f;
#pragma unroll
    for (int g = 0; g < 3; ++g) L.sacc[sw][g * 64 + c * 8 + r] = rs8(acc + g * 8, b3);
    if (lane == 0) { L.sm[sw] = M; L.sl[sw] = l; }
    if (sw == 0) STAMP(2);
    if (sw == 7) STAMP(9);
  } else {
    const int cw = wave;
    const unsigned ep = *(const unsigned*)(ws + WS_EPOCH);
    unsigned xw[5];
#pragma unroll
    for (int j = 0; j < 5; ++j) xw[j] = ((const unsigned*)x)[lane + 64 * j];
    u32x4 wqv[5][3];
#pragma unroll
    for (int g = 0; g < 5; ++g)
#pragma unroll
      for (int sg = 0; sg < 3; ++sg)
        wqv[g][sg] = ldnt(wq + (size_t)(80 * s + 40 * cw + g * 8 + r) * QD + DH * h + sg * 64 + c * 8);
    const int ks = b / 24, cb = b % 24;
    u32x4 w1v[4];
    if (b < 240) {
#pragma unroll
      for (int i = 0; i < 4; ++i) w1v[i] = ldnt(w1 + (size_t)(ks * 64 + (cw * 4 + i) * 8 + r) * F + cb * 64 + c * 8);
    }
    const unsigned tag = ep + 1u;
    float xv[10];
#pragma unroll
    for (int j = 0; j < 5; ++j) { xv[2 * j] = lo2f(xw[j]); xv[2 * j + 1] = hi2f(xw[j]); }
    {
      float sum = 0.f;
#pragma unroll
      for (int j = 0; j < 10; ++j) sum += xv[j];
      const float mean = wave_sum(sum) * (1.0f / D);
      float s2 = 0.f;
#pragma unroll
      for (int j = 0; j < 10; ++j) { const float d = xv[j] - mean; s2 += d * d; }
      const float var = wave_sum(s2) * (1.0f / D);
      const float mb = rbf(mean), den = rbf(sqrtf(rbf(rbf(var) + 1e-5f)));
#pragma unroll
      for (int j = 0; j < 10; ++j) xv[j] = rbf(rbf(xv[j] - mb) / den);
#pragma unroll
      for (int j = 0; j < 5; ++j) *(float2*)&L.xn[cw][2 * (lane + 64 * j)] = make_float2(xv[2 * j], xv[2 * j + 1]);
    }
    lds_fence();
    {
      float acc[24];
#pragma unroll
      for (int i = 0; i < 24; ++i) acc[i] = 0.f;
#pragma unroll
      for (int g = 0; g < 5; ++g) {
        const float xr = L.xn[cw][80 * s + 40 * cw + g * 8 + r];
#pragma unroll
        for (int sg = 0; sg < 3; ++sg)
#pragma unroll
          for (int jj = 0; jj < 4; ++jj) {
            acc[sg * 8 + 2 * jj] += xr * lo2f(wqv[g][sg][jj]);
            acc[sg * 8 + 2 * jj + 1] += xr * hi2f(wqv[g][sg][jj]);
          }
      }
#pragma unroll
      for (int sg = 0; sg < 3; ++sg)
        st_gran(ws + WS_QP, (unsigned)((h * 16 + s * 2 + cw) * 192 + sg * 64 + c * 8 + r), rs8(acc + sg * 8, b3), tag);
    }
    if (cw == 0) STAMP(3);
    {
      float sum = 0.f;
#pragma unroll
      for (int j = 0; j < 10; ++j) sum += xv[j];
      const float mean = wave_sum(sum) * (1.0f / D);
      float s2 = 0.f;
#pragma unroll
      for (int j = 0; j < 10; ++j) { const float d = xv[j] - mean; s2 += d * d; }
      const float var = wave_sum(s2) * (1.0f / D);
      const float mb = rbf(mean), den = rbf(sqrtf(rbf(rbf(var) + 1e-5f)));
#pragma unroll
      for (int j = 0; j < 5; ++j)
        *(float2*)&L.yy[cw][2 * (lane + 64 * j)] = make_float2(rbf(rbf(xv[2 * j] - mb) / den), rbf(rbf(xv[2 * j + 1] - mb) / den));
      lds_fence();
      if (b < 240) {
        float acc[8];
#pragma unroll
        for (int i = 0; i < 8; ++i) acc[i] = 0.f;
#pragma unroll
        for (int i = 0; i < 4; ++i) {
          const float yr = L.yy[cw][ks * 64 + (cw * 4 + i) * 8 + r];
#pragma unroll
          for (int jj = 0; jj < 4; ++jj) { acc[2 * jj] += yr * lo2f(w1v[i][jj]); acc[2 * jj + 1] += yr * hi2f(w1v[i][jj]); }
        }
        st_gran(ws + WS_HP, (unsigned)((ks * 2 + cw) * 1536 + cb * 64 + c * 8 + r), rs8(acc, b3), tag);
      }
    }
    {
      const unsigned off0 = (unsigned)(WS_QP + (size_t)(h * 16) * 192 * 8) + (unsigned)(cw * 48 + (lane < 48 ? lane : 47)) * 16u;
      u32x4 g[16];
#pragma unroll
      for (int p = 0; p < 16; ++p) g[p] = ld_gran2(rs, off0 + p * 1536u);
      for (int spin = 0; spin < SPIN_CAP; ++spin) {
        asm volatile("" ::: "memory");
        bool ok = true;
#pragma unroll
        for (int p = 0; p < 16; ++p) ok &= (g[p][1] == tag) & (g[p][3] == tag);
        if (__all(ok)) break;
        __builtin_amdgcn_s_sleep(1);
#pragma unroll
        for (int p = 0; p < 16; ++p) g[p] = ld_gran2(rs, off0 + p * 1536u);
      }
      float q0 = 0.f, q1 = 0.f;
#pragma unroll
      for (int p = 0; p < 16; ++p) { q0 += __uint_as_float(g[p][0]); q1 += __uint_as_float(g[p][2]); }
      if (lane < 48) L.qb[cw * 48 + lane] = (__builtin_bit_cast(unsigned, rbf(q0)) >> 16) | __builtin_bit_cast(unsigned, rbf(q1));
      if (lane == 0 && cw == 0) L.tag[0] = tag;
    }
    wg_barrier();
    if (cw == 0) STAMP(4);
    {
      u32x4 t[15];
#pragma unroll
      for (int i = 0; i < 15; ++i) {
        const int idx = cw * 960 + i * 64 + lane, row = idx / 10, ch = idx % 10;
        t[i] = ldnt(wo + (size_t)(DH * h + row) * D + 80 * s + ch * 8);
      }
      const int r6 = lane / 10, c10 = lane % 10;
      u32x4 w2v[8];
      if (cw == 1) {
#pragma unroll
        for (int i = 0; i < 8; ++i) w2v[i] = ldnt(w2 + (size_t)(48 * h + i * 6 + (lane < 60 ? r6 : 0)) * D + 80 * s + c10 * 8);
      }
#pragma unroll
      for (int i = 0; i < 15; ++i) *(u32x4*)&L.wo[(cw * 960 + i * 64 + lane) * 4] = t[i];
      if (cw == 1) {
        const int pr = lane % 24, pg = lane / 24;
        const unsigned off0 = (unsigned)WS_HP + (unsigned)((lane < 48 ? pg : 0) * 10 * 1536 + 48 * h + 2 * pr) * 8u;
        u32x4 g[10];
        for (int spin = 0; spin < SPIN_CAP; ++spin) {
        asm volatile("" ::: "memory");
          bool ok = true;
#pragma unroll
          for (int p = 0; p < 10; ++p) g[p] = ld_gran2(rs, off0 + p * 1536u * 8u);
#pragma unroll
          for (int p = 0; p < 10; ++p) ok &= (g[p][1] == tag) & (g[p][3] == tag);
          if (__all(ok)) break;
          __builtin_amdgcn_s_sleep(8);
        }
        float a0 = 0.f, a1 = 0.f;
#pragma unroll
        for (int p = 0; p < 10; ++p) { a0 += __uint_as_float(g[p][0]); a1 += __uint_as_float(g[p][2]); }
        const float o0 = __shfl(a0, lane + 24), o1 = __shfl(a1, lane + 24);
        if (lane < 24) {
          const unsigned bb = ((const unsigned*)b1)[24 * h + lane];
          float t0 = rbf(rbf(a0 + o0) + lo2f(bb)), t1 = rbf(rbf(a1 + o1) + hi2f(bb));
          float g0 = 0.5f * t0 * (1.0f + tanhf(0.7978845608028654f * (t0 + 0.044715f * t0 * t0 * t0)));
          float g1 = 0.5f * t1 * (1.0f + tanhf(0.7978845608028654f * (t1 + 0.044715f * t1 * t1 * t1)));
          *(float2*)&L.hh[2 * lane] = make_float2(rbf(g0), rbf(g1));
        }
        lds_fence();
        float acc[8];
#pragma unroll
        for (int i = 0; i < 8; ++i) acc[i] = 0.f;
#pragma unroll
        for (int i = 0; i < 8; ++i) {
          const float hv = L.hh[i * 6 + (lane < 60 ? r6 : 0)];
#pragma unroll
          for (int jj = 0; jj < 4; ++jj) { acc[2 * jj] += hv * lo2f(w2v[i][jj]); acc[2 * jj + 1] += hv * hi2f(w2v[i][jj]); }
        }
        if (lane < 60) {
          *(f32x4*)&L.m2red[r6][c10 * 8] = f32x4{acc[0], acc[1], acc[2], acc[3]};
          *(f32x4*)&L.m2red[r6][c10 * 8 + 4] = f32x4{acc[4], acc[5], acc[6], acc[7]};
        }
        lds_fence();
#pragma unroll
        for (int k = 0; k < 2; ++k) {
          const int col = lane + 64 * k;
          if (col < 80) L.ymlp[col] = L.m2red[0][col] + L.m2red[1][col] + L.m2red[2][col] + L.m2red[3][col] + L.m2red[4][col] + L.m2red[5][col];
        }
      }
    }
  }
  wg_barrier();
  const unsigned tag = L.tag[0];
  if (tid < 192) {
    float MM = L.sm[0];
#pragma unroll
    for (int i = 1; i < 8; ++i) MM = fmaxf(MM, L.sm[i]);
    float o = 0.f, ll = 0.f;
#pragma unroll
    for (int i = 0; i < 8; ++i) { const float e = __expf(L.sm[i] - MM); o += e * L.sacc[i][tid]; ll += e * L.sl[i]; }
    void* ap = ws + WS_AP + (size_t)(h * 8 + s) * 256 * 8;
    st_gran(ap, tid, o, tag);
    if (tid == 0) { st_gran(ap, 192, MM, tag); st_gran(ap, 193, ll, tag); }
  }
  if (tid == 0) STAMP(5);
  if (wave < 2) {
    const int pr = tid < 96 ? tid : 95;
    const unsigned off0 = (unsigned)(WS_AP + (size_t)(h * 8) * 256 * 8);
    u32x4 g[8], ml[8];
    for (int spin = 0; spin < SPIN_CAP; ++spin) {
        asm volatile("" ::: "memory");
      bool ok = true;
#pragma unroll
      for (int p = 0; p < 8; ++p) { g[p] = ld_gran2(rs, off0 + p * 2048u + pr * 16u); ml[p] = ld_gran2(rs, off0 + p * 2048u + 96 * 16u); }
#pragma unroll
      for (int p = 0; p < 8; ++p) ok &= (g[p][1] == tag) & (g[p][3] == tag) & (ml[p][1] == tag) & (ml[p][3] == tag);
      if (__all(ok)) break;
      __builtin_amdgcn_s_sleep(1);
    }
    float MM = __uint_as_float(ml[0][0]);
#pragma unroll
    for (int p = 1; p < 8; ++p) MM = fmaxf(MM, __uint_as_float(ml[p][0]));
    float o0 = 0.f, o1 = 0.f, ll = 0.f;
#pragma unroll
    for (int p = 0; p < 8; ++p) {
      const float e = __expf(__uint_as_float(ml[p][0]) - MM);
      o0 += e * __uint_as_float(g[p][0]); o1 += e * __uint_as_float(g[p][2]); ll += e * __uint_as_float(ml[p][2]);
    }
    const float inv = 1.0f / ll;
    if (tid < 96) *(float2*)&L.o[2 * tid] = make_float2(rbf(o0 * inv), rbf(o1 * inv));
  }
  wg_barrier();
  if (tid == 0) STAMP(6);
  {
    const int cp = tid % 40, rg = tid / 40;
    float a0 = 0.f, a1 = 0.f;
#pragma unroll
    for (int i = 0; i < 12; ++i) {
      const float ov = L.o[rg * 12 + i];
      const unsigned w = L.wo[(rg * 12 + i) * 40 + cp];
      a0 += ov * lo2f(w); a1 += ov * hi2f(w);
    }
    *(float2*)&L.red[rg][2 * cp] = make_float2(a0, a1);
  }
  wg_barrier();
  if (tid < 160) {
    float v;
    if (tid < 80) {
      v = 0.f;
#pragma unroll
      for (int i = 0; i < 16; ++i) v += L.red[i][tid];
    } else v = L.ymlp[tid - 80];
    st_gran(ws + WS_YP + (size_t)(s * 32 + h) * 160 * 8, tid, v, tag);
  }
  if (tid == 0) STAMP(7);
  if (h != 0) return;
  {
    const int pr = tid % 80, pg = tid / 80;
    const unsigned off0 = (unsigned)(WS_YP + (size_t)(s * 32 + pg * 4) * 160 * 8) + pr * 16u;
    u32x4 g[4];
    for (int spin = 0; spin < SPIN_CAP; ++spin) {
        asm volatile("" ::: "memory");
      bool ok = true;
#pragma unroll
      for (int p = 0; p < 4; ++p) g[p] = ld_gran2(rs, off0 + p * 1280u);
#pragma unroll
      for (int p = 0; p < 4; ++p) ok &= (g[p][1] == tag) & (g[p][3] == tag);
      if (__all(ok)) break;
      __builtin_amdgcn_s_sleep(1);
    }
    float a0 = 0.f, a1 = 0.f;
#pragma unroll
    for (int p = 0; p < 4; ++p) { a0 += __uint_as_float(g[p][0]); a1 += __uint_as_float(g[p][2]); }
    *(float2*)&L.fin[pg][2 * pr] = make_float2(a0, a1);
  }
  wg_barrier();
  if (tid < 80) {
    float sa = 0.f, t = 0.f;
#pragma unroll
    for (int i = 0; i < 8; ++i) { sa += L.fin[i][tid]; t += L.fin[i][80 + tid]; }
    const int col = 80 * s + tid;
    sa = rbf(sa);
    t = rbf(rbf(t) + lo2f(b2[col]));
    const float g = rbf(t / (1.0f + __expf(-t)));
    const float res = rbf(lo2f(x[col]) + rbf(sa * g));
    out[col] = (unsigned short)(__builtin_bit_cast(unsigned, res) >> 16);
    if (tid == 0 && s == 0) *(unsigned*)(ws + WS_EPOCH) = tag;
  }
  if (tid == 0) STAMP(8);
}

extern "C" void kernel_launch(void* const* d_in, const int* in_sizes, int n_in, void* d_out, int out_size, void* d_ws,
                              size_t ws_size, hipStream_t stream) {
  fused_kernel<<<256, NTH, 0, stream>>>((const unsigned short*)d_in[0], (const u32x4*)d_in[1], (const u32x4*)d_in[2],
                                         (const unsigned short*)d_in[3], (const unsigned short*)d_in[4],
                                         (const unsigned short*)d_in[6], (const unsigned short*)d_in[7],
                                         (const unsigned short*)d_in[8], (const unsigned short*)d_in[9],
                                         (unsigned short*)d_out, (unsigned char*)d_ws);
}
```

```cpp
#include <hip/hip_runtime.h>
#include <hip/hip_bf16.h>
#include <stdint.h>

#define PD 4
#define PB1 2
#define PACE 1
#define STAMPS 0

constexpr int D = 640, H = 32, SK = 8192, DH = 192, F = 1536, QD = H * DH;
constexpr int NSW = 8, NCW = 2, NTH = (NSW + NCW) * 64;
constexpr int NU = 128 / NSW;

using u32x4 = __attribute__((ext_vector_type(4))) unsigned;
using u32x2 = __attribute__((ext_vector_type(2))) unsigned;
using f32x4 = __attribute__((ext_vector_type(4))) float;
using bf16x2 = __attribute__((ext_vector_type(2))) __bf16;
typedef __attribute__((address_space(1))) unsigned long long gu64;
typedef __attribute__((address_space(1))) unsigned gu32;

constexpr size_t WS_EPOCH = 0;
constexpr size_t WS_QP = 4096;
constexpr size_t WS_HP = WS_QP + (size_t)32 * 16 * 192 * 8;
constexpr size_t WS_AP = WS_HP + (size_t)20 * 1536 * 8;
constexpr size_t WS_YP = WS_AP + (size_t)32 * 8 * 256 * 8;
constexpr size_t WS_END = WS_YP + (size_t)8 * 32 * 160 * 8;
constexpr size_t WS_STAMP = (size_t)1 << 26;

__device__ __forceinline__ float lo2f(unsigned u) { return __builtin_bit_cast(float, u << 16); }
__device__ __forceinline__ float hi2f(unsigned u) { return __builtin_bit_cast(float, u & 0xffff0000u); }
__device__ __forceinline__ float rbf(float f) {
  unsigned u = __builtin_bit_cast(unsigned, f);
  u = (u + 0x7FFFu + ((u >> 16) & 1u)) & 0xFFFF0000u;
  return __builtin_bit_cast(float, u);
}
__device__ __forceinline__ float dot2(unsigned k, unsigned q, float acc) {
  return __builtin_amdgcn_fdot2_f32_bf16(__builtin_bit_cast(bf16x2, k), __builtin_bit_cast(bf16x2, q), acc, false);
}
template <int CTRL> __device__ __forceinline__ float dpp(float x) {
  return __builtin_bit_cast(float, __builtin_amdgcn_mov_dpp(__builtin_bit_cast(int, x), CTRL, 0xf, 0xf, true));
}
constexpr int XOR1 = 0xB1, XOR2 = 0x4E, XOR7 = 0x141, XOR8 = 0x128;
__device__ __forceinline__ float swap16_sum(float a, float b) {
  auto s = __builtin_amdgcn_permlane16_swap(__float_as_uint(a), __float_as_uint(b), false, false);
  return __uint_as_float(s[0]) + __uint_as_float(s[1]);
}
__device__ __forceinline__ float swap32_sum(float a, float b) {
  auto s = __builtin_amdgcn_permlane32_swap(__float_as_uint(a), __float_as_uint(b), false, false);
  return __uint_as_float(s[0]) + __uint_as_float(s[1]);
}
__device__ __forceinline__ float swap16_max(float a) {
  auto s = __builtin_amdgcn_permlane16_swap(__float_as_uint(a), __float_as_uint(a), false, false);
  return fmaxf(__uint_as_float(s[0]), __uint_as_float(s[1]));
}
__device__ __forceinline__ float swap32_max(float a) {
  auto s = __builtin_amdgcn_permlane32_swap(__float_as_uint(a), __float_as_uint(a), false, false);
  return fmaxf(__uint_as_float(s[0]), __uint_as_float(s[1]));
}
__device__ __forceinline__ float wave_sum(float v) {
  v += dpp<XOR1>(v); v += dpp<XOR2>(v); v += dpp<XOR7>(v); v += dpp<XOR8>(v);
  v = swap16_sum(v, v); v = swap32_sum(v, v);
  return v;
}
__device__ __forceinline__ float rs8(const float* a, bool b3) {
  float v1[4], v2[2];
#pragma unroll
  for (int j = 0; j < 4; ++j) v1[j] = swap32_sum(a[j], a[j + 4]);
#pragma unroll
  for (int j = 0; j < 2; ++j) v2[j] = swap16_sum(v1[j], v1[j + 2]);
  float t = b3 ? v2[0] : v2[1], keep = b3 ? v2[1] : v2[0];
  return keep + dpp<XOR8>(t);
}
__device__ __forceinline__ void wg_barrier() { asm volatile("s_waitcnt lgkmcnt(0)\n\ts_barrier" ::: "memory"); }
__device__ __forceinline__ void lds_fence() { asm volatile("s_waitcnt lgkmcnt(0)" ::: "memory"); }
__device__ __forceinline__ u32x4 ldnt(const void* p) { return __builtin_nontemporal_load((const u32x4*)p); }
__device__ __forceinline__ void st_gran(void* base, unsigned idx, float v, unsigned tag) {
  __hip_atomic_store((gu64*)base + idx, ((unsigned long long)tag << 32) | __builtin_bit_cast(unsigned, v), __ATOMIC_RELAXED, __HIP_MEMORY_SCOPE_AGENT);
}
__device__ __forceinline__ u32x4 ld_gran2(__amdgpu_buffer_rsrc_t rs, unsigned byte_off) {
  return __builtin_amdgcn_raw_buffer_load_b128(rs, byte_off, 0, 16);
}
constexpr int SPIN_CAP = 1 << 20;

struct __attribute__((aligned(16))) Lds {
  float xn[2][640];
  float yy[2][640];
  unsigned qb[96];
  unsigned tag[4];
  float sm[8], sl[8];
  float sacc[8][192];
  float o[192];
  unsigned wo[192 * 40];
  float red[16][80];
  float m2red[6][80];
  float ymlp[80];
  float hh[48];
  float fin[8][160];
};

__global__ void __launch_bounds__(NTH, 1)
fused_kernel(const unsigned short* __restrict__ x, const u32x4* __restrict__ kc, const u32x4* __restrict__ vc,
             const unsigned short* __restrict__ wq, const unsigned short* __restrict__ wo,
             const unsigned short* __restrict__ w1, const unsigned short* __restrict__ b1,
             const unsigned short* __restrict__ w2, const unsigned short* __restrict__ b2,
             unsigned short* __restrict__ out, unsigned char* __restrict__ ws) {
  __shared__ Lds L;
  const int b = blockIdx.x, xcd = b & 7, jb = b >> 3, h = xcd * 4 + (jb >> 3), s = jb & 7;
  const int tid = threadIdx.x, lane = tid & 63, wave = __builtin_amdgcn_readfirstlane(tid >> 6);
  const int r = lane >> 3, c = lane & 7;
  const bool b3 = (lane & 8) != 0;
  const __amdgpu_buffer_rsrc_t rs = __builtin_amdgcn_make_buffer_rsrc((void*)ws, 0, (int)WS_END, 0x00020000);
#define STAMP(i) do {} while (0)

  if (wave >= NCW) {
    const int sw = wave - NCW;
    const size_t base = ((size_t)h * SK + (size_t)s * 1024) * 24 + r * 24 + c;
    const u32x4* kp = kc + base; const u32x4* vp = vc + base;
    u32x4 kb[PD][3], vb[PD][3];
#define UNIT(i) (sw + NSW * (i))
#define ISSUE(i) do { const int o_ = UNIT(i) * 192; \
    _Pragma("unroll") for (int g = 0; g < 3; ++g) kb[(i) % PD][g] = ldnt(kp + o_ + g * 8); \
    _Pragma("unroll") for (int g = 0; g < 3; ++g) vb[(i) % PD][g] = ldnt(vp + o_ + g * 8); } while (0)
    if (sw == 0) STAMP(0);
#pragma unroll
    for (int i = 0; i < PB1; ++i) ISSUE(i);
#pragma unroll
    for (int i = PB1; i < PD; ++i) {
      asm volatile("s_waitcnt vmcnt(%0)" :: "n"(6 * (PB1 - 1)) : "memory");
      ISSUE(i);
    }
    wg_barrier();
    if (sw == 0) STAMP(1);
    unsigned qp[12];
#pragma unroll
    for (int g = 0; g < 3; ++g) { u32x4 t = *(const u32x4*)&L.qb[g * 32 + c * 4]; qp[g * 4] = t[0]; qp[g * 4 + 1] = t[1]; qp[g * 4 + 2] = t[2]; qp[g * 4 + 3] = t[3]; }
    float m = -INFINITY, l = 0.f, acc[24];
#pragma unroll
    for (int i = 0; i < 24; ++i) acc[i] = 0.f;
    const float scale = 0.07216878364870322f;
#pragma unroll
    for (int i = 0; i < NU; ++i) {
      const int sl_ = i % PD;
      float d0 = 0.f, d1 = 0.f, d2 = 0.f;
#pragma unroll
      for (int jj = 0; jj < 4; ++jj) { d0 = dot2(kb[sl_][0][jj], qp[jj], d0); d1 = dot2(kb[sl_][1][jj], qp[4 + jj], d1); d2 = dot2(kb[sl_][2][jj], qp[8 + jj], d2); }
      float d = d0 + d1 + d2;
      d += dpp<XOR1>(d); d += dpp<XOR2>(d); d += dpp<XOR7>(d);
      const float sc = rbf(rbf(d) * scale);
      const float mn = fmaxf(m, sc);
      const float al = __expf(m - mn), p = __expf(sc - mn);
      l = l * al + p; m = mn;
      u32x4 vv[3] = {vb[sl_][0], vb[sl_][1], vb[sl_][2]};
      if (i + PD < NU) ISSUE(i + PD);
#pragma unroll
      for (int g = 0; g < 3; ++g)
#pragma unroll
        for (int jj = 0; jj < 4; ++jj) {
          const int ii = g * 8 + jj * 2;
          acc[ii] = acc[ii] * al + p * lo2f(vv[g][jj]);
          acc[ii + 1] = acc[ii + 1] * al + p * hi2f(vv[g][jj]);
        }
    }
    float M = m;
    M = fmaxf(M, dpp<XOR8>(M)); M = swap16_max(M); M = swap32_max(M);
    const float f = __expf(m - M);
    l *= f;
    l += dpp<XOR8>(l); l = swap16_sum(l, l); l = swap32_sum(l, l);
#pragma unroll
    for (int i = 0; i < 24; ++i) acc[i] *= f;
#pragma unroll
    for (int g = 0; g < 3; ++g) L.sacc[sw][g * 64 + c * 8 + r] = rs8(acc + g * 8, b3);
    if (lane == 0) { L.sm[sw] = M; L.sl[sw] = l; }
    if (sw == 0) STAMP(2);
    if (sw == 7) STAMP(9);
  } else {
    const int cw = wave;
    const unsigned ep = *(const unsigned*)(ws + WS_EPOCH);
    unsigned xw[5];
#pragma unroll
    for (int j = 0; j < 5; ++j) xw[j] = ((const unsigned*)x)[lane + 64 * j];
    u32x4 wqv[5][3];
#pragma unroll
    for (int g = 0; g < 5; ++g)
#pragma unroll
      for (int sg = 0; sg < 3; ++sg)
        wqv[g][sg] = ldnt(wq + (size_t)(80 * s + 40 * cw + g * 8 + r) * QD + DH * h + sg * 64 + c * 8);
    const int ks = b / 24, cb = b % 24;
    u32x4 w1v[4];
    if (b < 240) {
#pragma unroll
      for (int i = 0; i < 4; ++i) w1v[i] = ldnt(w1 + (size_t)(ks * 64 + (cw * 4 + i) * 8 + r) * F + cb * 64 + c * 8);
    }
    const unsigned tag = ep + 1u;
    float xv[10];
#pragma unroll
    for (int j = 0; j < 5; ++j) { xv[2 * j] = lo2f(xw[j]); xv[2 * j + 1] = hi2f(xw[j]); }
    {
      float sum = 0.f;
#pragma unroll
      for (int j = 0; j < 10; ++j) sum += xv[j];
      const float mean = wave_sum(sum) * (1.0f / D);
      float s2 = 0.f;
#pragma unroll
      for (int j = 0; j < 10; ++j) { const float d = xv[j] - mean; s2 += d * d; }
      const float var = wave_sum(s2) * (1.0f / D);
      const float mb = rbf(mean), den = rbf(sqrtf(rbf(rbf(var) + 1e-5f)));
#pragma unroll
      for (int j = 0; j < 10; ++j) xv[j] = rbf(rbf(xv[j] - mb) / den);
#pragma unroll
      for (int j = 0; j < 5; ++j) *(float2*)&L.xn[cw][2 * (lane + 64 * j)] = make_float2(xv[2 * j], xv[2 * j + 1]);
    }
    lds_fence();
    {
      float acc[24];
#pragma unroll
      for (int i = 0; i < 24; ++i) acc[i] = 0.f;
#pragma unroll
      for (int g = 0; g < 5; ++g) {
        const float xr = L.xn[cw][80 * s + 40 * cw + g * 8 + r];
#pragma unroll
        for (int sg = 0; sg < 3; ++sg)
#pragma unroll
          for (int jj = 0; jj < 4; ++jj) {
            acc[sg * 8 + 2 * jj] += xr * lo2f(wqv[g][sg][jj]);
            acc[sg * 8 + 2 * jj + 1] += xr * hi2f(wqv[g][sg][jj]);
          }
      }
#pragma unroll
      for (int sg = 0; sg < 3; ++sg)
        st_gran(ws + WS_QP, (unsigned)((h * 16 + s * 2 + cw) * 192 + sg * 64 + c * 8 + r), rs8(acc + sg * 8, b3), tag);
    }
    if (cw == 0) STAMP(3);
    {
      float sum = 0.f;
#pragma unroll
      for (int j = 0; j < 10; ++j) sum += xv[j];
      const float mean = wave_sum(sum) * (1.0f / D);
      float s2 = 0.f;
#pragma unroll
      for (int j = 0; j < 10; ++j) { const float d = xv[j] - mean; s2 += d * d; }
      const float var = wave_sum(s2) * (1.0f / D);
      const float mb = rbf(mean), den = rbf(sqrtf(rbf(rbf(var) + 1e-5f)));
#pragma unroll
      for (int j = 0; j < 5; ++j)
        *(float2*)&L.yy[cw][2 * (lane + 64 * j)] = make_float2(rbf(rbf(xv[2 * j] - mb) / den), rbf(rbf(xv[2 * j + 1] - mb) / den));
      lds_fence();
      if (b < 240) {
        float acc[8];
#pragma unroll
        for (int i = 0; i < 8; ++i) acc[i] = 0.f;
#pragma unroll
        for (int i = 0; i < 4; ++i) {
          const float yr = L.yy[cw][ks * 64 + (cw * 4 + i) * 8 + r];
#pragma unroll
          for (int jj = 0; jj < 4; ++jj) { acc[2 * jj] += yr * lo2f(w1v[i][jj]); acc[2 * jj + 1] += yr * hi2f(w1v[i][jj]); }
        }
        st_gran(ws + WS_HP, (unsigned)((ks * 2 + cw) * 1536 + cb * 64 + c * 8 + r), rs8(acc, b3), tag);
      }
    }
    {
      const unsigned off0 = (unsigned)(WS_QP + (size_t)(h * 16) * 192 * 8) + (unsigned)(cw * 48 + (lane < 48 ? lane : 47)) * 16u;
      u32x4 g[16];
#pragma unroll
      for (int p = 0; p < 16; ++p) g[p] = ld_gran2(rs, off0 + p * 1536u);
      for (int spin = 0; spin < SPIN_CAP; ++spin) {
        asm volatile("" ::: "memory");
        bool ok = true;
#pragma unroll
        for (int p = 0; p < 16; ++p) ok &= (g[p][1] == tag) & (g[p][3] == tag);
        if (__all(ok)) break;
        __builtin_amdgcn_s_sleep(1);
#pragma unroll
        for (int p = 0; p < 16; ++p) g[p] = ld_gran2(rs, off0 + p * 1536u);
      }
      float q0 = 0.f, q1 = 0.f;
#pragma unroll
      for (int p = 0; p < 16; ++p) { q0 += __uint_as_float(g[p][0]); q1 += __uint_as_float(g[p][2]); }
      if (lane < 48) L.qb[cw * 48 + lane] = (__builtin_bit_cast(unsigned, rbf(q0)) >> 16) | __builtin_bit_cast(unsigned, rbf(q1));
      if (lane == 0 && cw == 0) L.tag[0] = tag;
    }
    wg_barrier();
    if (cw == 0) STAMP(4);
    {
      u32x4 t[15];
#pragma unroll
      for (int i = 0; i < 15; ++i) {
        const int idx = cw * 960 + i * 64 + lane, row = idx / 10, ch = idx % 10;
        t[i] = ldnt(wo + (size_t)(DH * h + row) * D + 80 * s + ch * 8);
      }
      const int r6 = lane / 10, c10 = lane % 10;
      u32x4 w2v[8];
      if (cw == 1) {
#pragma unroll
        for (int i = 0; i < 8; ++i) w2v[i] = ldnt(w2 + (size_t)(48 * h + i * 6 + (lane < 60 ? r6 : 0)) * D + 80 * s + c10 * 8);
      }
#pragma unroll
      for (int i = 0; i < 15; ++i) *(u32x4*)&L.wo[(cw * 960 + i * 64 + lane) * 4] = t[i];
      if (cw == 1) {
        const int pr = lane % 24, pg = lane / 24;
        const unsigned off0 = (unsigned)WS_HP + (unsigned)((lane < 48 ? pg : 0) * 10 * 1536 + 48 * h + 2 * pr) * 8u;
        u32x4 g[10];
        for (int spin = 0; spin < SPIN_CAP; ++spin) {
        asm volatile("" ::: "memory");
          bool ok = true;
#pragma unroll
          for (int p = 0; p < 10; ++p) g[p] = ld_gran2(rs, off0 + p * 1536u * 8u);
#pragma unroll
          for (int p = 0; p < 10; ++p) ok &= (g[p][1] == tag) & (g[p][3] == tag);
          if (__all(ok)) break;
          __builtin_amdgcn_s_sleep(8);
        }
        float a0 = 0.f, a1 = 0.f;
#pragma unroll
        for (int p = 0; p < 10; ++p) { a0 += __uint_as_float(g[p][0]); a1 += __uint_as_float(g[p][2]); }
        const float o0 = __shfl(a0, lane + 24), o1 = __shfl(a1, lane + 24);
        if (lane < 24) {
          const unsigned bb = ((const unsigned*)b1)[24 * h + lane];
          float t0 = rbf(rbf(a0 + o0) + lo2f(bb)), t1 = rbf(rbf(a1 + o1) + hi2f(bb));
          float g0 = 0.5f * t0 * (1.0f + tanhf(0.7978845608028654f * (t0 + 0.044715f * t0 * t0 * t0)));
          float g1 = 0.5f * t1 * (1.0f + tanhf(0.7978845608028654f * (t1 + 0.044715f * t1 * t1 * t1)));
          *(float2*)&L.hh[2 * lane] = make_float2(rbf(g0), rbf(g1));
        }
        lds_fence();
        float acc[8];
#pragma unroll
        for (int i = 0; i < 8; ++i) acc[i] = 0.f;
#pragma unroll
        for (int i = 0; i < 8; ++i) {
          const float hv = L.hh[i * 6 + (lane < 60 ? r6 : 0)];
#pragma unroll
          for (int jj = 0; jj < 4; ++jj) { acc[2 * jj] += hv * lo2f(w2v[i][jj]); acc[2 * jj + 1] += hv * hi2f(w2v[i][jj]); }
        }
        if (lane < 60) {
          *(f32x4*)&L.m2red[r6][c10 * 8] = f32x4{acc[0], acc[1], acc[2], acc[3]};
          *(f32x4*)&L.m2red[r6][c10 * 8 + 4] = f32x4{acc[4], acc[5], acc[6], acc[7]};
        }
        lds_fence();
#pragma unroll
        for (int k = 0; k < 2; ++k) {
          const int col = lane + 64 * k;
          if (col < 80) L.ymlp[col] = L.m2red[0][col] + L.m2red[1][col] + L.m2red[2][col] + L.m2red[3][col] + L.m2red[4][col] + L.m2red[5][col];
        }
      }
    }
  }
  wg_barrier();
  const unsigned tag = L.tag[0];
  if (tid < 192) {
    float MM = L.sm[0];
#pragma unroll
    for (int i = 1; i < 8; ++i) MM = fmaxf(MM, L.sm[i]);
    float o = 0.f, ll = 0.f;
#pragma unroll
    for (int i = 0; i < 8; ++i) { const float e = __expf(L.sm[i] - MM); o += e * L.sacc[i][tid]; ll += e * L.sl[i]; }
    void* ap = ws + WS_AP + (size_t)(h * 8 + s) * 256 * 8;
    st_gran(ap, tid, o, tag);
    if (tid == 0) { st_gran(ap, 192, MM, tag); st_gran(ap, 193, ll, tag); }
  }
  if (tid == 0) STAMP(5);
  if (wave < 2) {
    const int pr = tid < 96 ? tid : 95;
    const unsigned off0 = (unsigned)(WS_AP + (size_t)(h * 8) * 256 * 8);
    u32x4 g[8], ml[8];
    for (int spin = 0; spin < SPIN_CAP; ++spin) {
        asm volatile("" ::: "memory");
      bool ok = true;
#pragma unroll
      for (int p = 0; p < 8; ++p) { g[p] = ld_gran2(rs, off0 + p * 2048u + pr * 16u); ml[p] = ld_gran2(rs, off0 + p * 2048u + 96 * 16u); }
#pragma unroll
      for (int p = 0; p < 8; ++p) ok &= (g[p][1] == tag) & (g[p][3] == tag) & (ml[p][1] == tag) & (ml[p][3] == tag);
      if (__all(ok)) break;
      __builtin_amdgcn_s_sleep(1);
    }
    float MM = __uint_as_float(ml[0][0]);
#pragma unroll
    for (int p = 1; p < 8; ++p) MM = fmaxf(MM, __uint_as_float(ml[p][0]));
    float o0 = 0.f, o1 = 0.f, ll = 0.f;
#pragma unroll
    for (int p = 0; p < 8; ++p) {
      const float e = __expf(__uint_as_float(ml[p][0]) - MM);
      o0 += e * __uint_as_float(g[p][0]); o1 += e * __uint_as_float(g[p][2]); ll += e * __uint_as_float(ml[p][2]);
    }
    const float inv = 1.0f / ll;
    if (tid < 96) *(float2*)&L.o[2 * tid] = make_float2(rbf(o0 * inv), rbf(o1 * inv));
  }
  wg_barrier();
  if (tid == 0) STAMP(6);
  {
    const int cp = tid % 40, rg = tid / 40;
    float a0 = 0.f, a1 = 0.f;
#pragma unroll
    for (int i = 0; i < 12; ++i) {
      const float ov = L.o[rg * 12 + i];
      const unsigned w = L.wo[(rg * 12 + i) * 40 + cp];
      a0 += ov * lo2f(w); a1 += ov * hi2f(w);
    }
    *(float2*)&L.red[rg][2 * cp] = make_float2(a0, a1);
  }
  wg_barrier();
  if (tid < 160) {
    float v;
    if (tid < 80) {
      v = 0.f;
#pragma unroll
      for (int i = 0; i < 16; ++i) v += L.red[i][tid];
    } else v = L.ymlp[tid - 80];
    st_gran(ws + WS_YP + (size_t)(s * 32 + h) * 160 * 8, tid, v, tag);
  }
  if (tid == 0) STAMP(7);
  if (h != 0) return;
  {
    const int pr = tid % 80, pg = tid / 80;
    const unsigned off0 = (unsigned)(WS_YP + (size_t)(s * 32 + pg * 4) * 160 * 8) + pr * 16u;
    u32x4 g[4];
    for (int spin = 0; spin < SPIN_CAP; ++spin) {
        asm volatile("" ::: "memory");
      bool ok = true;
#pragma unroll
      for (int p = 0; p < 4; ++p) g[p] = ld_gran2(rs, off0 + p * 1280u);
#pragma unroll
      for (int p = 0; p < 4; ++p) ok &= (g[p][1] == tag) & (g[p][3] == tag);
      if (__all(ok)) break;
      __builtin_amdgcn_s_sleep(1);
    }
    float a0 = 0.f, a1 = 0.f;
#pragma unroll
    for (int p = 0; p < 4; ++p) { a0 += __uint_as_float(g[p][0]); a1 += __uint_as_float(g[p][2]); }
    *(float2*)&L.fin[pg][2 * pr] = make_float2(a0, a1);
  }
  wg_barrier();
  if (tid < 80) {
    float sa = 0.f, t = 0.f;
#pragma unroll
    for (int i = 0; i < 8; ++i) { sa += L.fin[i][tid]; t += L.fin[i][80 + tid]; }
    const int col = 80 * s + tid;
    sa = rbf(sa);
    t = rbf(rbf(t) + lo2f(b2[col]));
    const float g = rbf(t / (1.0f + __expf(-t)));
    const float res = rbf(lo2f(x[col]) + rbf(sa * g));
    out[col] = (unsigned short)(__builtin_bit_cast(unsigned, res) >> 16);
    if (tid == 0 && s == 0) *(unsigned*)(ws + WS_EPOCH) = tag;
  }
  if (tid == 0) STAMP(8);
}

extern "C" void kernel_launch(void* const* d_in, const int* in_sizes, int n_in, void* d_out, int out_size, void* d_ws,
                              size_t ws_size, hipStream_t stream) {
  fused_kernel<<<256, NTH, 0, stream>>>((const unsigned short*)d_in[0], (const u32x4*)d_in[1], (const u32x4*)d_in[2],
                                         (const unsigned short*)d_in[3], (const unsigned short*)d_in[4],
                                         (const unsigned short*)d_in[6], (const unsigned short*)d_in[7],
                                         (const unsigned short*)d_in[8], (const unsigned short*)d_in[9],
                                         (unsigned short*)d_out, (unsigned char*)d_ws);
}
```

```cpp
#include <hip/hip_runtime.h>
#include <hip/hip_bf16.h>
#include <stdint.h>

#define PD 4
#define PB1 1
#define PACE 1

constexpr int D = 640, H = 32, SK = 8192, DH = 192, F = 1536, QD = H * DH;
constexpr int NSW = 8, NCW = 2, NTH = (NSW + NCW) * 64;
constexpr int NU = 128 / NSW;

using u32x4 = __attribute__((ext_vector_type(4))) unsigned;
using u32x2 = __attribute__((ext_vector_type(2))) unsigned;
using f32x4 = __attribute__((ext_vector_type(4))) float;
using bf16x2 = __attribute__((ext_vector_type(2))) __bf16;
typedef __attribute__((address_space(1))) unsigned long long gu64;
typedef __attribute__((address_space(1))) unsigned gu32;

constexpr size_t WS_EPOCH = 0;
constexpr size_t WS_QP = 4096;
constexpr size_t WS_HP = WS_QP + (size_t)32 * 16 * 192 * 8;
constexpr size_t WS_AP = WS_HP + (size_t)20 * 1536 * 8;
constexpr size_t WS_YP = WS_AP + (size_t)32 * 8 * 256 * 8;
constexpr size_t WS_END = WS_YP + (size_t)8 * 32 * 160 * 8;

__device__ __forceinline__ float lo2f(unsigned u) { return __builtin_bit_cast(float, u << 16); }
__device__ __forceinline__ float hi2f(unsigned u) { return __builtin_bit_cast(float, u & 0xffff0000u); }
__device__ __forceinline__ float rbf(float f) {
  unsigned u = __builtin_bit_cast(unsigned, f);
  u = (u + 0x7FFFu + ((u >> 16) & 1u)) & 0xFFFF0000u;
  return __builtin_bit_cast(float, u);
}
__device__ __forceinline__ float dot2(unsigned k, unsigned q, float acc) {
  return __builtin_amdgcn_fdot2_f32_bf16(__builtin_bit_cast(bf16x2, k), __builtin_bit_cast(bf16x2, q), acc, false);
}
template <int CTRL> __device__ __forceinline__ float dpp(float x) {
  return __builtin_bit_cast(float, __builtin_amdgcn_mov_dpp(__builtin_bit_cast(int, x), CTRL, 0xf, 0xf, true));
}
constexpr int XOR1 = 0xB1, XOR2 = 0x4E, XOR7 = 0x141, XOR8 = 0x128;
__device__ __forceinline__ float swap16_sum(float a, float b) {
  auto s = __builtin_amdgcn_permlane16_swap(__float_as_uint(a), __float_as_uint(b), false, false);
  return __uint_as_float(s[0]) + __uint_as_float(s[1]);
}
__device__ __forceinline__ float swap32_sum(float a, float b) {
  auto s = __builtin_amdgcn_permlane32_swap(__float_as_uint(a), __float_as_uint(b), false, false);
  return __uint_as_float(s[0]) + __uint_as_float(s[1]);
}
__device__ __forceinline__ float swap16_max(float a) {
  auto s = __builtin_amdgcn_permlane16_swap(__float_as_uint(a), __float_as_uint(a), false, false);
  return fmaxf(__uint_as_float(s[0]), __uint_as_float(s[1]));
}
__device__ __forceinline__ float swap32_max(float a) {
  auto s = __builtin_amdgcn_permlane32_swap(__float_as_uint(a), __float_as_uint(a), false, false);
  return fmaxf(__uint_as_float(s[0]), __uint_as_float(s[1]));
}
__device__ __forceinline__ float wave_sum(float v) {
  v += dpp<XOR1>(v); v += dpp<XOR2>(v); v += dpp<XOR7>(v); v += dpp<XOR8>(v);
  v = swap16_sum(v, v); v = swap32_sum(v, v);
  return v;
}
__device__ __forceinline__ float rs8(const float* a, bool b3) {
  float v1[4], v2[2];
#pragma unroll
  for (int j = 0; j < 4; ++j) v1[j] = swap32_sum(a[j], a[j + 4]);
#pragma unroll
  for (int j = 0; j < 2; ++j) v2[j] = swap16_sum(v1[j], v1[j + 2]);
  float t = b3 ? v2[0] : v2[1], keep = b3 ? v2[1] : v2[0];
  return keep + dpp<XOR8>(t);
}
__device__ __forceinline__ void wg_barrier() { asm volatile("s_waitcnt lgkmcnt(0)\n\ts_barrier" ::: "memory"); }
__device__ __forceinline__ void lds_fence() { asm volatile("s_waitcnt lgkmcnt(0)" ::: "memory"); }
__device__ __forceinline__ u32x4 ldnt(const void* p) { return __builtin_nontemporal_load((const u32x4*)p); }
__device__ __forceinline__ void st_gran(void* base, unsigned idx, float v, unsigned tag) {
  __hip_atomic_store((gu64*)base + idx, ((unsigned long long)tag << 32) | __builtin_bit_cast(unsigned, v), __ATOMIC_RELAXED, __HIP_MEMORY_SCOPE_AGENT);
}
__device__ __forceinline__ u32x4 ld_gran2(__amdgpu_buffer_rsrc_t rs, unsigned byte_off) {
  return __builtin_amdgcn_raw_buffer_load_b128(rs, byte_off, 0, 16);
}
constexpr int SPIN_CAP = 1 << 20;

struct __attribute__((aligned(16))) Lds {
  float xn[2][640];
  float yy[2][640];
  unsigned qb[96];
  unsigned tag[4];
  float sm[8], sl[8];
  float sacc[8][192];
  float o[192];
  unsigned wo[192 * 40];
  float red[16][80];
  float m2red[6][80];
  float ymlp[80];
  float hh[48];
  float fin[8][160];
};

__global__ void __launch_bounds__(NTH, 1)
fused_kernel(const unsigned short* __restrict__ x, const u32x4* __restrict__ kc, const u32x4* __restrict__ vc,
             const unsigned short* __restrict__ wq, const unsigned short* __restrict__ wo,
             const unsigned short* __restrict__ w1, const unsigned short* __restrict__ b1,
             const unsigned short* __restrict__ w2, const unsigned short* __restrict__ b2,
             unsigned short* __restrict__ out, unsigned char* __restrict__ ws) {
  __shared__ Lds L;
  const int b = blockIdx.x, xcd = b & 7, jb = b >> 3, h = xcd * 4 + (jb >> 3), s = jb & 7;
  const int tid = threadIdx.x, lane = tid & 63, wave = __builtin_amdgcn_readfirstlane(tid >> 6);
  const int r = lane >> 3, c = lane & 7;
  const bool b3 = (lane & 8) != 0;
  const __amdgpu_buffer_rsrc_t rs = __builtin_amdgcn_make_buffer_rsrc((void*)ws, 0, (int)WS_END, 0x00020000);

  if (wave >= NCW) {
    const int sw = wave - NCW;
    const size_t base = ((size_t)h * SK + (size_t)s * 1024) * 24 + r * 24 + c;
    const u32x4* kp = kc + base; const u32x4* vp = vc + base;
    u32x4 kb[PD][3], vb[PD][3];
#define UNIT(i) (sw + NSW * (i))
#define ISSUE(i) do { const int o_ = UNIT(i) * 192; \
    _Pragma("unroll") for (int g = 0; g < 3; ++g) kb[(i) % PD][g] = ldnt(kp + o_ + g * 8); \
    _Pragma("unroll") for (int g = 0; g < 3; ++g) vb[(i) % PD][g] = ldnt(vp + o_ + g * 8); } while (0)
#pragma unroll
    for (int i = 0; i < PB1; ++i) ISSUE(i);
#pragma unroll
    for (int i = PB1; i < PD; ++i) {
      asm volatile("s_waitcnt vmcnt(%0)" :: "n"(6 * (PB1 - 1)) : "memory");
      ISSUE(i);
    }
    wg_barrier();
    unsigned qp[12];
#pragma unroll
    for (int g = 0; g < 3; ++g) { u32x4 t = *(const u32x4*)&L.qb[g * 32 + c * 4]; qp[g * 4] = t[0]; qp[g * 4 + 1] = t[1]; qp[g * 4 + 2] = t[2]; qp[g * 4 + 3] = t[3]; }
    float m = -INFINITY, l = 0.f, acc[24];
#pragma unroll
    for (int i = 0; i < 24; ++i) acc[i] = 0.f;
    const float scale = 0.07216878364870322f;
#pragma unroll
    for (int i = 0; i < NU; ++i) {
      const int sl_ = i % PD;
      float d0 = 0.f, d1 = 0.f, d2 = 0.f;
#pragma unroll
      for (int jj = 0; jj < 4; ++jj) { d0 = dot2(kb[sl_][0][jj], qp[jj], d0); d1 = dot2(kb[sl_][1][jj], qp[4 + jj], d1); d2 = dot2(kb[sl_][2][jj], qp[8 + jj], d2); }
      float d = d0 + d1 + d2;
      d += dpp<XOR1>(d); d += dpp<XOR2>(d); d += dpp<XOR7>(d);
      const float sc = rbf(rbf(d) * scale);
      const float mn = fmaxf(m, sc);
      const float al = __expf(m - mn), p = __expf(sc - mn);
      l = l * al + p; m = mn;
      u32x4 vv[3] = {vb[sl_][0], vb[sl_][1], vb[sl_][2]};
      if (i + PD < NU) ISSUE(i + PD);
#pragma unroll
      for (int g = 0; g < 3; ++g)
#pragma unroll
        for (int jj = 0; jj < 4; ++jj) {
          const int ii = g * 8 + jj * 2;
          acc[ii] = acc[ii] * al + p * lo2f(vv[g][jj]);
          acc[ii + 1] = acc[ii + 1] * al + p * hi2f(vv[g][jj]);
        }
    }
    float M = m;
    M = fmaxf(M, dpp<XOR8>(M)); M = swap16_max(M); M = swap32_max(M);
    const float f = __expf(m - M);
    l *= f;
    l += dpp<XOR8>(l); l = swap16_sum(l, l); l = swap32_sum(l, l);
#pragma unroll
    for (int i = 0; i < 24; ++i) acc[i] *= f;
#pragma unroll
    for (int g = 0; g < 3; ++g) L.sacc[sw][g * 64 + c * 8 + r] = rs8(acc + g * 8, b3);
    if (lane == 0) { L.sm[sw] = M; L.sl[sw] = l; }
  } else {
    const int cw = wave;
    const unsigned ep = *(const unsigned*)(ws + WS_EPOCH);
    unsigned xw[5];
#pragma unroll
    for (int j = 0; j < 5; ++j) xw[j] = ((const unsigned*)x)[lane + 64 * j];
    u32x4 wqv[5][3];
#pragma unroll
    for (int g = 0; g < 5; ++g)
#pragma unroll
      for (int sg = 0; sg < 3; ++sg)
        wqv[g][sg] = ldnt(wq + (size_t)(80 * s + 40 * cw + g * 8 + r) * QD + DH * h + sg * 64 + c * 8);
    const int ks = b / 24, cb = b % 24;
    u32x4 w1v[4];
    if (b < 240) {
#pragma unroll
      for (int i = 0; i < 4; ++i) w1v[i] = ldnt(w1 + (size_t)(ks * 64 + (cw * 4 + i) * 8 + r) * F + cb * 64 + c * 8);
    }
    const unsigned tag = ep + 1u;
    float xv[10];
#pragma unroll
    for (int j = 0; j < 5; ++j) { xv[2 * j] = lo2f(xw[j]); xv[2 * j + 1] = hi2f(xw[j]); }
    {
      float sum = 0.f;
#pragma unroll
      for (int j = 0; j < 10; ++j) sum += xv[j];
      const float mean = wave_sum(sum) * (1.0f / D);
      float s2 = 0.f;
#pragma unroll
      for (int j = 0; j < 10; ++j) { const float d = xv[j] - mean; s2 += d * d; }
      const float var = wave_sum(s2) * (1.0f / D);
      const float mb = rbf(mean), den = rbf(sqrtf(rbf(rbf(var) + 1e-5f)));
#pragma unroll
      for (int j = 0; j < 10; ++j) xv[j] = rbf(rbf(xv[j] - mb) / den);
#pragma unroll
      for (int j = 0; j < 5; ++j) *(float2*)&L.xn[cw][2 * (lane + 64 * j)] = make_float2(xv[2 * j], xv[2 * j + 1]);
    }
    lds_fence();
    {
      float acc[24];
#pragma unroll
      for (int i = 0; i < 24; ++i) acc[i] = 0.f;
#pragma unroll
      for (int g = 0; g < 5; ++g) {
        const float xr = L.xn[cw][80 * s + 40 * cw + g * 8 + r];
#pragma unroll
        for (int sg = 0; sg < 3; ++sg)
#pragma unroll
          for (int jj = 0; jj < 4; ++jj) {
            acc[sg * 8 + 2 * jj] += xr * lo2f(wqv[g][sg][jj]);
            acc[sg * 8 + 2 * jj + 1] += xr * hi2f(wqv[g][sg][jj]);
          }
      }
#pragma unroll
      for (int sg = 0; sg < 3; ++sg)
        st_gran(ws + WS_QP, (unsigned)((h * 16 + s * 2 + cw) * 192 + sg * 64 + c * 8 + r), rs8(acc + sg * 8, b3), tag);
    }
    {
      float sum = 0.f;
#pragma unroll
      for (int j = 0; j < 10; ++j) sum += xv[j];
      const float mean = wave_sum(sum) * (1.0f / D);
      float s2 = 0.f;
#pragma unroll
      for (int j = 0; j < 10; ++j) { const float d = xv[j] - mean; s2 += d * d; }
      const float var = wave_sum(s2) * (1.0f / D);
      const float mb = rbf(mean), den = rbf(sqrtf(rbf(rbf(var) + 1e-5f)));
#pragma unroll
      for (int j = 0; j < 5; ++j)
        *(float2*)&L.yy[cw][2 * (lane + 64 * j)] = make_float2(rbf(rbf(xv[2 * j] - mb) / den), rbf(rbf(xv[2 * j + 1] - mb) / den));
      lds_fence();
      if (b < 240) {
        float acc[8];
#pragma unroll
        for (int i = 0; i < 8; ++i) acc[i] = 0.f;
#pragma unroll
        for (int i = 0; i < 4; ++i) {
          const float yr = L.yy[cw][ks * 64 + (cw * 4 + i) * 8 + r];
#pragma unroll
          for (int jj = 0; jj < 4; ++jj) { acc[2 * jj] += yr * lo2f(w1v[i][jj]); acc[2 * jj + 1] += yr * hi2f(w1v[i][jj]); }
        }
        st_gran(ws + WS_HP, (unsigned)((ks * 2 + cw) * 1536 + cb * 64 + c * 8 + r), rs8(acc, b3), tag);
      }
    }
    {
      const unsigned off0 = (unsigned)(WS_QP + (size_t)(h * 16) * 192 * 8) + (unsigned)(cw * 48 + (lane < 48 ? lane : 47)) * 16u;
      u32x4 g[16];
#pragma unroll
      for (int p = 0; p < 16; ++p) g[p] = ld_gran2(rs, off0 + p * 1536u);
      for (int spin = 0; spin < SPIN_CAP; ++spin) {
        asm volatile("" ::: "memory");
        bool ok = true;
#pragma unroll
        for (int p = 0; p < 16; ++p) ok &= (g[p][1] == tag) & (g[p][3] == tag);
        if (__all(ok)) break;
        __builtin_amdgcn_s_sleep(1);
#pragma unroll
        for (int p = 0; p < 16; ++p) g[p] = ld_gran2(rs, off0 + p * 1536u);
      }
      float q0 = 0.f, q1 = 0.f;
#pragma unroll
      for (int p = 0; p < 16; ++p) { q0 += __uint_as_float(g[p][0]); q1 += __uint_as_float(g[p][2]); }
      if (lane < 48) L.qb[cw * 48 + lane] = (__builtin_bit_cast(unsigned, rbf(q0)) >> 16) | __builtin_bit_cast(unsigned, rbf(q1));
      if (lane == 0 && cw == 0) L.tag[0] = tag;
    }
    wg_barrier();
    {
      u32x4 t[15];
#pragma unroll
      for (int i = 0; i < 15; ++i) {
        const int idx = cw * 960 + i * 64 + lane, row = idx / 10, ch = idx % 10;
        t[i] = ldnt(wo + (size_t)(DH * h + row) * D + 80 * s + ch * 8);
      }
      const int r6 = lane / 10, c10 = lane % 10;
      u32x4 w2v[8];
      if (cw == 1) {
#pragma unroll
        for (int i = 0; i < 8; ++i) w2v[i] = ldnt(w2 + (size_t)(48 * h + i * 6 + (lane < 60 ? r6 : 0)) * D + 80 * s + c10 * 8);
      }
#pragma unroll
      for (int i = 0; i < 15; ++i) *(u32x4*)&L.wo[(cw * 960 + i * 64 + lane) * 4] = t[i];
      if (cw == 1) {
        const int pr = lane % 24, pg = lane / 24;
        const unsigned off0 = (unsigned)WS_HP + (unsigned)((lane < 48 ? pg : 0) * 10 * 1536 + 48 * h + 2 * pr) * 8u;
        u32x4 g[10];
        for (int spin = 0; spin < SPIN_CAP; ++spin) {
        asm volatile("" ::: "memory");
          bool ok = true;
#pragma unroll
          for (int p = 0; p < 10; ++p) g[p] = ld_gran2(rs, off0 + p * 1536u * 8u);
#pragma unroll
          for (int p = 0; p < 10; ++p) ok &= (g[p][1] == tag) & (g[p][3] == tag);
          if (__all(ok)) break;
          __builtin_amdgcn_s_sleep(8);
        }
        float a0 = 0.f, a1 = 0.f;
#pragma unroll
        for (int p = 0; p < 10; ++p) { a0 += __uint_as_float(g[p][0]); a1 += __uint_as_float(g[p][2]); }
        const float o0 = __shfl(a0, lane + 24), o1 = __shfl(a1, lane + 24);
        if (lane < 24) {
          const unsigned bb = ((const unsigned*)b1)[24 * h + lane];
          float t0 = rbf(rbf(a0 + o0) + lo2f(bb)), t1 = rbf(rbf(a1 + o1) + hi2f(bb));
          float g0 = 0.5f * t0 * (1.0f + tanhf(0.7978845608028654f * (t0 + 0.044715f * t0 * t0 * t0)));
          float g1 = 0.5f * t1 * (1.0f + tanhf(0.7978845608028654f * (t1 + 0.044715f * t1 * t1 * t1)));
          *(float2*)&L.hh[2 * lane] = make_float2(rbf(g0), rbf(g1));
        }
        lds_fence();
        float acc[8];
#pragma unroll
        for (int i = 0; i < 8; ++i) acc[i] = 0.f;
#pragma unroll
        for (int i = 0; i < 8; ++i) {
          const float hv = L.hh[i * 6 + (lane < 60 ? r6 : 0)];
#pragma unroll
          for (int jj = 0; jj < 4; ++jj) { acc[2 * jj] += hv * lo2f(w2v[i][jj]); acc[2 * jj + 1] += hv * hi2f(w2v[i][jj]); }
        }
        if (lane < 60) {
          *(f32x4*)&L.m2red[r6][c10 * 8] = f32x4{acc[0], acc[1], acc[2], acc[3]};
          *(f32x4*)&L.m2red[r6][c10 * 8 + 4] = f32x4{acc[4], acc[5], acc[6], acc[7]};
        }
        lds_fence();
#pragma unroll
        for (int k = 0; k < 2; ++k) {
          const int col = lane + 64 * k;
          if (col < 80) L.ymlp[col] = L.m2red[0][col] + L.m2red[1][col] + L.m2red[2][col] + L.m2red[3][col] + L.m2red[4][col] + L.m2red[5][col];
        }
      }
    }
  }
  wg_barrier();
  const unsigned tag = L.tag[0];
  if (tid < 192) {
    float MM = L.sm[0];
#pragma unroll
    for (int i = 1; i < 8; ++i) MM = fmaxf(MM, L.sm[i]);
    float o = 0.f, ll = 0.f;
#pragma unroll
    for (int i = 0; i < 8; ++i) { const float e = __expf(L.sm[i] - MM); o += e * L.sacc[i][tid]; ll += e * L.sl[i]; }
    void* ap = ws + WS_AP + (size_t)(h * 8 + s) * 256 * 8;
    st_gran(ap, tid, o, tag);
    if (tid == 0) { st_gran(ap, 192, MM, tag); st_gran(ap, 193, ll, tag); }
  }
  if (wave < 2) {
    const int pr = tid < 96 ? tid : 95;
    const unsigned off0 = (unsigned)(WS_AP + (size_t)(h * 8) * 256 * 8);
    u32x4 g[8], ml[8];
    for (int spin = 0; spin < SPIN_CAP; ++spin) {
        asm volatile("" ::: "memory");
      bool ok = true;
#pragma unroll
      for (int p = 0; p < 8; ++p) { g[p] = ld_gran2(rs, off0 + p * 2048u + pr * 16u); ml[p] = ld_gran2(rs, off0 + p * 2048u + 96 * 16u); }
#pragma unroll
      for (int p = 0; p < 8; ++p) ok &= (g[p][1] == tag) & (g[p][3] == tag) & (ml[p][1] == tag) & (ml[p][3] == tag);
      if (__all(ok)) break;
      __builtin_amdgcn_s_sleep(1);
    }
    float MM = __uint_as_float(ml[0][0]);
#pragma unroll
    for (int p = 1; p < 8; ++p) MM = fmaxf(MM, __uint_as_float(ml[p][0]));
    float o0 = 0.f, o1 = 0.f, ll = 0.f;
#pragma unroll
    for (int p = 0; p < 8; ++p) {
      const float e = __expf(__uint_as_float(ml[p][0]) - MM);
      o0 += e * __uint_as_float(g[p][0]); o1 += e * __uint_as_float(g[p][2]); ll += e * __uint_as_float(ml[p][2]);
    }
    const float inv = 1.0f / ll;
    if (tid < 96) *(float2*)&L.o[2 * tid] = make_float2(rbf(o0 * inv), rbf(o1 * inv));
  }
  wg_barrier();
  {
    const int cp = tid % 40, rg = tid / 40;
    float a0 = 0.f, a1 = 0.f;
#pragma unroll
    for (int i = 0; i < 12; ++i) {
      const float ov = L.o[rg * 12 + i];
      const unsigned w = L.wo[(rg * 12 + i) * 40 + cp];
      a0 += ov * lo2f(w); a1 += ov * hi2f(w);
    }
    *(float2*)&L.red[rg][2 * cp] = make_float2(a0, a1);
  }
  wg_barrier();
  if (tid < 160) {
    float v;
    if (tid < 80) {
      v = 0.f;
#pragma unroll
      for (int i = 0; i < 16; ++i) v += L.red[i][tid];
    } else v = L.ymlp[tid - 80];
    st_gran(ws + WS_YP + (size_t)(s * 32 + h) * 160 * 8, tid, v, tag);
  }
  if (h != 0) return;
  {
    const int pr = tid % 80, pg = tid / 80;
    const unsigned off0 = (unsigned)(WS_YP + (size_t)(s * 32 + pg * 4) * 160 * 8) + pr * 16u;
    u32x4 g[4];
    for (int spin = 0; spin < SPIN_CAP; ++spin) {
        asm volatile("" ::: "memory");
      bool ok = true;
#pragma unroll
      for (int p = 0; p < 4; ++p) g[p] = ld_gran2(rs, off0 + p * 1280u);
#pragma unroll
      for (int p = 0; p < 4; ++p) ok &= (g[p][1] == tag) & (g[p][3] == tag);
      if (__all(ok)) break;
      __builtin_amdgcn_s_sleep(1);
    }
    float a0 = 0.f, a1 = 0.f;
#pragma unroll
    for (int p = 0; p < 4; ++p) { a0 += __uint_as_float(g[p][0]); a1 += __uint_as_float(g[p][2]); }
    *(float2*)&L.fin[pg][2 * pr] = make_float2(a0, a1);
  }
  wg_barrier();
  if (tid < 80) {
    float sa = 0.f, t = 0.f;
#pragma unroll
    for (int i = 0; i < 8; ++i) { sa += L.fin[i][tid]; t += L.fin[i][80 + tid]; }
    const int col = 80 * s + tid;
    sa = rbf(sa);
    t = rbf(rbf(t) + lo2f(b2[col]));
    const float g = rbf(t / (1.0f + __expf(-t)));
    const float res = rbf(lo2f(x[col]) + rbf(sa * g));
    out[col] = (unsigned short)(__builtin_bit_cast(unsigned, res) >> 16);
    if (tid == 0 && s == 0) *(unsigned*)(ws + WS_EPOCH) = tag;
  }
}

extern "C" void kernel_launch(void* const* d_in, const int* in_sizes, int n_in, void* d_out, int out_size, void* d_ws,
                              size_t ws_size, hipStream_t stream) {
  fused_kernel<<<256, NTH, 0, stream>>>((const unsigned short*)d_in[0], (const u32x4*)d_in[1], (const u32x4*)d_in[2],
                                         (const unsigned short*)d_in[3], (const unsigned short*)d_in[4],
                                         (const unsigned short*)d_in[6], (const unsigned short*)d_in[7],
                                         (const unsigned short*)d_in[8], (const unsigned short*)d_in[9],
                                         (unsigned short*)d_out, (unsigned char*)d_ws);
}
```
